```python
import torch

def _softmax(x, axis=-1):
    return torch.softmax(x, dim=axis)
def _gelu(x):
    return torch.nn.functional.gelu(x, approximate="tanh")
def _silu(x):
    return torch.nn.functional.silu(x)
def _sigmoid(x):
    return torch.sigmoid(x)
def _layernorm(x, axis=-1, eps=1e-5):
    m = x.mean(dim=axis, keepdim=True)
    v = x.var(dim=axis, keepdim=True, unbiased=False)
    return (x - m) / torch.sqrt(v + eps)
def _scatter_add(tgt, idx, src, axis=0):
    return tgt.clone().index_add_(axis, idx, src)
def _segment_sum(src, seg, n):
    out = torch.zeros((n,) + src.shape[1:], dtype=src.dtype)
    return out.index_add_(0, seg, src)
def _pad(x, pw):
    flat = tuple(v for pair in reversed(pw) for v in pair)
    return torch.nn.functional.pad(x, flat)

kernel_name = 'concat_heads__softmax_full_mha__grouped_kv_mqa__prompt'

def setup_inputs():

    _w = (lambda a: (lambda t: t.to(torch.bfloat16) if t.is_floating_point() else t)(torch.as_tensor(a)))
    def _A_setup():
        B, S, D = 1, 2048, 640
        return {
            "x": _w((lambda *s: torch.randn(*s, dtype=torch.bfloat16) * 0.2)(B, S, D)),
            "wqkv": _w((lambda *s: torch.randn(*s, dtype=torch.bfloat16) * 0.2)(D, 3 * D)),
            "wo": _w((lambda *s: torch.randn(*s, dtype=torch.bfloat16) * 0.05)(D, D)),
        }
    _A_in = _A_setup()

    def _B_setup():
        B, S, D, H = 1, 2048, 640, 32
        Dh = D // H
        return {
            "x": _w((lambda *s: torch.randn(*s, dtype=torch.bfloat16) * 0.05)(B, S, D)),
            "wq": _w((lambda *s: torch.randn(*s, dtype=torch.bfloat16) * 0.8)(D, H * Dh)),
            "wk": _w((lambda *s: torch.randn(*s, dtype=torch.bfloat16) * 0.8)(D, Dh)),
            "wv": _w((lambda *s: torch.randn(*s, dtype=torch.bfloat16) * 0.1)(D, Dh)),
            "wo": _w((lambda *s: torch.randn(*s, dtype=torch.bfloat16) * 0.1)(H * Dh, D)),
        }
    _B_in = _B_setup()

    return {
        "A_x": _A_in["x"],
        "A_wqkv": _A_in["wqkv"],
        "A_wo": _A_in["wo"],
        "B_x": _B_in["x"],
        "B_wq": _B_in["wq"],
        "B_wk": _B_in["wk"],
        "B_wv": _B_in["wv"],
        "B_wo": _B_in["wo"],
    }

def reference(A_x, A_wqkv, A_wo, B_x, B_wq, B_wk, B_wv, B_wo):
    def _A_ref(x, wqkv, wo):
        B, S, D = x.shape
        H = 32; Dh = D // H
        qkv = (lambda x, *d: x.permute(*d))((x @ wqkv).reshape(B, S, 3, H, Dh), 2, 0, 3, 1, 4)
        q, k, v = qkv[0], qkv[1], qkv[2]
        scale = Dh ** -0.5
        scores = torch.einsum("bhqd,bhkd->bhqk", q, k) * scale
        p = _softmax(scores, axis=-1)
        o = torch.einsum("bhqk,bhkd->bhqd", p, v)
        return (lambda x, *d: x.permute(*d))(o, 0, 2, 1, 3).reshape(B, S, D) @ wo

    def _B_ref(x, wq, wk, wv, wo):
        B, S, D = x.shape
        H = 32
        Dh = D // H
        q = (lambda x, *d: x.permute(*d))((x @ wq).reshape(B, S, H, Dh), 0, 2, 1, 3)
        k = x @ wk
        v = x @ wv
        scores = torch.einsum("bhqd,bkd->bhqk", q, k) * (Dh ** -0.5)
        p = _softmax(scores, axis=-1)
        o = torch.einsum("bhqk,bkd->bhqd", p, v)
        return (lambda x, *d: x.permute(*d))(o, 0, 2, 1, 3).reshape(B, S, H * Dh) @ wo

    a = _A_ref(A_x, A_wqkv, A_wo)
    b = _B_ref(B_x, B_wq, B_wk, B_wv, B_wo)
    return torch.concatenate([a, b], axis=-1)
```

```cpp
#include <hip/hip_runtime.h>
#include <hip/hip_bf16.h>
#include <stdint.h>

typedef unsigned short u16;
using bf16x8 = __attribute__((ext_vector_type(8))) short;
using bf16x4 = __attribute__((ext_vector_type(4))) short;
using f32x16 = __attribute__((ext_vector_type(16))) float;
using u32x4 = __attribute__((ext_vector_type(4))) unsigned;
using u32x2 = __attribute__((ext_vector_type(2))) unsigned;
using f32x4 = __attribute__((ext_vector_type(4))) float;
typedef float f32x2 __attribute__((ext_vector_type(2)));
typedef __bf16 bfv2 __attribute__((ext_vector_type(2)));
typedef short v4i16_t __attribute__((ext_vector_type(4)));
#define DI __device__ __forceinline__
#define LDSP __attribute__((address_space(3)))

constexpr int S = 2048, D = 640, H = 32, DH = 20;
constexpr int NT = S / 32;
constexpr int RB = 22;
constexpr int VOFF = 1296;
constexpr int VS = 2 * RB * 16;
constexpr int REC = VOFF + 4 * RB * 16;
constexpr int QREC = 1280;
constexpr size_t WS_KVA = 0;
constexpr size_t WS_KVB = WS_KVA + (size_t)H * NT * REC;
constexpr size_t WS_Q = WS_KVB + (size_t)NT * REC + 4096;
constexpr size_t WS_O = WS_Q + (size_t)2 * H * NT * QREC + 4096;
constexpr size_t WS_END = WS_O + (size_t)2 * S * D * 2;
constexpr float QSCALE = 0.22360679774997896f * 1.4426950408889634f;

DI unsigned cvtpk(float lo, float hi) { f32x2 v = {lo, hi}; bfv2 b = __builtin_convertvector(v, bfv2); return __builtin_bit_cast(unsigned, b); }
DI void glds16(const void* g, unsigned lds_base) {
  unsigned sv;
  asm volatile("s_mov_b32 %0, m0\n\ts_mov_b32 m0, %2\n\ts_nop 0\n\tglobal_load_lds_dwordx4 %1, off\n\ts_mov_b32 m0, %0" : "=&s"(sv) : "v"(g), "s"(lds_base) : "memory");
}
DI void glds16s(const void* sbase, unsigned voff, unsigned lds_base) {
  unsigned sv;
  asm volatile("s_mov_b32 %0, m0\n\ts_mov_b32 m0, %3\n\ts_nop 0\n\tglobal_load_lds_dwordx4 %1, %2\n\ts_mov_b32 m0, %0" : "=&s"(sv) : "v"(voff), "s"(sbase), "s"(lds_base) : "memory");
}
#define MFMA16(a, b, c) __builtin_amdgcn_mfma_f32_32x32x16_bf16(a, b, c, 0, 0, 0)
#define MFMA8(a, b, c) __builtin_amdgcn_mfma_f32_32x32x8bf16_1k(a, b, c, 0, 0, 0)
#define SB() __builtin_amdgcn_sched_barrier(0)
#define PIN(x) asm volatile("" : "+v"(x))
DI unsigned rfl(unsigned x) { return (unsigned)__builtin_amdgcn_readfirstlane((int)x); }
DI bf16x4 trread(unsigned a) { return __builtin_bit_cast(bf16x4, __builtin_amdgcn_ds_read_tr16_b64_v4i16((LDSP v4i16_t*)a)); }

constexpr int BK = 64, NKC = D / BK, NST = 4;
constexpr int WROW = 320;
constexpr int WBYTES = BK * WROW;
constexpr int EBYTES = 3072;
DI int colmap_head(int ct, int g) {
  const int w = 4 * ct + 2 * (g >> 2) + (g & 1), u = (g >> 1) & 1, p = w / 5, e = w - 5 * p;
  return e < 4 ? 40 * p + 20 * (e >> 1) + 8 * (e & 1) + 4 * u : 40 * p + 20 * u + 16;
}
DI int colmap_oct(int ct, int g) { return 32 * ct + 16 * (g >> 2) + 8 * (g & 1) + 4 * ((g >> 1) & 1); }
DI int colmap_id(int ct, int g) { return 32 * ct + 4 * g; }

#define PROF_BEGIN(k)
#define PROF_MARK1(k)
#define PROF_MARK2(k)
#define PROF_END(k, wsp)
#define WAITV(n) asm volatile("s_waitcnt vmcnt(" #n ")" ::: "memory")
#define BARRIER() asm volatile("s_waitcnt lgkmcnt(0)\n\ts_barrier" ::: "memory")

template <int KIND, int EXTRA>
DI void proj_tile(const u16* __restrict__ x, const u16* __restrict__ w, int ldw, const u16* __restrict__ we, int row0,
                  char* qk_base  , size_t head_stride, int rec, char* e_base, char* lds, char* wsp) {
  PROF_BEGIN(1)
  constexpr int XBYTES = 128 * 128;
  constexpr int STG = XBYTES + WBYTES + EBYTES;
  const int tid = threadIdx.x, lane = tid & 63, r = lane & 31, h = lane >> 5;
  const int wid = __builtin_amdgcn_readfirstlane(tid >> 6);
  const int tt = wid & 3, cg = wid >> 2;
  const unsigned lds0 = (unsigned)(uintptr_t)lds;
  unsigned xo[2], wo[3];
#pragma unroll
  for (int i = 0; i < 2; ++i) { const int row = 8 * (2 * wid + i) + (lane >> 3); xo[i] = row * (D * 2) + (((lane & 7) ^ ((row >> 1) & 7)) << 4); }
  const int wj0 = cg == 0 ? 3 * wid : 12 + 2 * (wid - 4);
#pragma unroll
  for (int i = 0; i < 3; ++i) { const int p = 64 * (wj0 + i) + lane, row = p / 20, c = p - 20 * row; wo[i] = row * ldw * 2 + c * 16; }
  const char* xs = (const char*)x + (size_t)row0 * D * 2;
  const bool has_e = EXTRA != 0 && wid >= 4 && wid < 7;
  const bool n5 = cg == 0 || has_e;
  auto issue = [&](int c) {
    const unsigned sb = lds0 + (c % NST) * STG;
    const char* xc = xs + c * (BK * 2); const char* wc = (const char*)w + (size_t)c * BK * ldw * 2;
    glds16s(xc, xo[0], sb + (2 * wid) * 1024); glds16s(xc, xo[1], sb + (2 * wid + 1) * 1024);
    glds16s(wc, wo[0], sb + XBYTES + wj0 * 1024); glds16s(wc, wo[1], sb + XBYTES + (wj0 + 1) * 1024);
    if (cg == 0) glds16s(wc, wo[2], sb + XBYTES + (wj0 + 2) * 1024);
    if (EXTRA != 0 && has_e) {
      const char* ec = (const char*)we + c * 2560 + (wid - 4) * 1024;
      glds16s(ec, (wid == 6 ? (lane & 31) : lane) * 16, sb + XBYTES + WBYTES + (wid - 4) * 1024);
    }
  };
  issue(0); issue(1); issue(2);
  const unsigned xa = tt * 4096 + r * 128 + ((h ^ ((r >> 1) & 7)) << 4);
  const int g = 4 * ((lane >> 4) & 1) + (lane & 3), q = (lane & 15) >> 2;
  constexpr int NCT = 3;
  unsigned wa[NCT];
#pragma unroll
  for (int i = 0; i < NCT; ++i) {
    const int ct = cg * 3 + i;
    if (i == 2 && cg == 1) {
      int col;
      if (EXTRA == 1) { const int w8 = 2 * (g >> 2) + (g & 1), u = (g >> 1) & 1; col = w8 < 2 ? 8 * w8 + 4 * u : (w8 == 2 && u == 0 ? 16 : 0); }
      else col = g < 5 ? 4 * g : 0;
      wa[i] = XBYTES + WBYTES + (8 * h + q) * 40 + col * 2;
    } else {
      const int col = KIND == 2 ? colmap_id(ct, g) : colmap_head(ct, g);
      wa[i] = XBYTES + (8 * h + q) * WROW + col * 2;
    }
  }
  const bool third = cg == 0 || EXTRA != 0;
  f32x16 acc[NCT];
#pragma unroll
  for (int i = 0; i < NCT; ++i) acc[i] = f32x16{};
  for (int c = 0; c < NKC; ++c) {
    if (c < NKC - 2) { if (n5) WAITV(10); else WAITV(8); }
    else if (c == NKC - 2) { if (n5) WAITV(5); else WAITV(4); }
    else WAITV(0);
    BARRIER();
    if (c == 0) { PROF_MARK1(1) }
    if (c + 3 < NKC) issue(c + 3);
    const unsigned sb = lds0 + (c % NST) * STG;
#pragma unroll
    for (int ks = 0; ks < 4; ++ks) {
      const bf16x8 xf = *(const LDSP bf16x8*)(sb + (xa ^ (ks << 5)));
#pragma unroll
      for (int i = 0; i < NCT; ++i) {
        if (i == 2 && !third) continue;
        const bool ex = (i == 2 && cg == 1);
        const unsigned rs = ex ? 40 : WROW;
        const unsigned a = sb + wa[i] + (16 * ks) * rs;
        const bf16x4 lo = trread(a), hi = trread(a + 4 * rs);
        const bf16x8 wf = __builtin_shufflevector(lo, hi, 0, 1, 2, 3, 4, 5, 6, 7);
        const bool vor = ex ? (EXTRA == 2) : (KIND == 2);
        acc[i] = vor ? MFMA16(xf, wf, acc[i]) : MFMA16(wf, xf, acc[i]);
      }
    }
  }
  PROF_MARK2(1)
  const int tile = (row0 >> 5) + tt;
#pragma unroll
  for (int i = 0; i < NCT; ++i) {
    if (i == 2 && !third) continue;
    const bool ex = (i == 2 && cg == 1);
    const int ct = cg * 3 + i;
    if (ex ? (EXTRA == 2) : (KIND == 2)) {
      const int c = ex ? r : 32 * ct + r;
      const int hl = ex ? 0 : c / 20, d = ex ? r : c - 20 * hl;
      if (!ex || r < 20) {
        char* rb = (ex ? e_base : qk_base + hl * head_stride) + (size_t)tile * REC + VOFF + (h * RB) * 16;
#pragma unroll
        for (int s = 0; s < 2; ++s) {
          u32x4 v = {cvtpk(acc[i][8 * s], acc[i][8 * s + 1]), cvtpk(acc[i][8 * s + 2], acc[i][8 * s + 3]), cvtpk(acc[i][8 * s + 4], acc[i][8 * s + 5]), cvtpk(acc[i][8 * s + 6], acc[i][8 * s + 7])};
          *(u32x4*)(rb + s * VS + d * 16) = v;
          if (d == 0) {
            *(u32x4*)(rb + s * VS + 20 * 16) = u32x4{0x3F803F80u, 0x3F803F80u, 0x3F803F80u, 0x3F803F80u};
            *(u32x4*)(rb + s * VS + 21 * 16) = u32x4{0u, 0u, 0u, 0u};
          }
        }
      }
    } else {
      const bool isq = !ex && KIND == 0;
      const float sc = isq ? QSCALE : 1.0f;
#pragma unroll
      for (int o = 0; o < 2; ++o) {
        u32x4 v = {cvtpk(acc[i][8 * o] * sc, acc[i][8 * o + 1] * sc), cvtpk(acc[i][8 * o + 2] * sc, acc[i][8 * o + 3] * sc),
                   cvtpk(acc[i][8 * o + 4] * sc, acc[i][8 * o + 5] * sc), cvtpk(acc[i][8 * o + 6] * sc, acc[i][8 * o + 7] * sc)};
        if (ex) {
          char* rb = e_base + (size_t)tile * REC;
          if (o == 0) *(u32x4*)(rb + h * 512 + r * 16) = v;
          else if (h == 0) { *(u32x2*)(rb + 1024 + r * 8) = u32x2{v[0], v[1]}; if (r == 0) *(u32x2*)(rb + 1280) = u32x2{0x3F803F80u, 0x00003F80u}; }
        } else {
          const int w8 = 4 * ct + 2 * o + h, p = w8 / 5, e = w8 - 5 * p;
          char* rb = qk_base + (size_t)(2 * p) * head_stride + (size_t)tile * rec;
          if (e < 4) *(u32x4*)(rb + (e >> 1) * head_stride + (e & 1) * 512 + r * 16) = v;
          else {
            *(u32x2*)(rb + 1024 + r * 8) = u32x2{v[0], v[1]};
            *(u32x2*)(rb + head_stride + 1024 + r * 8) = u32x2{v[2], v[3]};
            if (KIND == 1 && r == 0) { *(u32x2*)(rb + 1280) = u32x2{0x3F803F80u, 0x00003F80u}; *(u32x2*)(rb + head_stride + 1280) = u32x2{0x3F803F80u, 0x00003F80u}; }
          }
        }
      }
    }
  }
  PROF_END(1, wsp)
}
constexpr int PROJ_LDS = NST * (128 * 128 + WBYTES + EBYTES);

__global__ __launch_bounds__(512) void proj_kernel(const u16* __restrict__ xa, const u16* __restrict__ wqkv, const u16* __restrict__ xb, const u16* __restrict__ wq,
                                                   const u16* __restrict__ wk, const u16* __restrict__ wv, char* __restrict__ ws) {
  extern __shared__ __attribute__((aligned(16))) char lds[];
  const int xg = blockIdx.x & 7, j = blockIdx.x >> 3;
  const int rblk = (xg & 1) * 8 + (j & 7), cq = j >> 3;
  const int row0 = rblk * 128;
  if (xg < 6) {
    const int kind = xg >> 1;
    const u16* w = wqkv + kind * 640 + cq * 160;
    if (kind == 0) proj_tile<0, 0>(xa, w, 1920, nullptr, row0, ws + WS_Q + (size_t)(cq * 8) * NT * QREC, (size_t)NT * QREC, QREC, nullptr, lds, ws);
    else if (kind == 1) proj_tile<1, 0>(xa, w, 1920, nullptr, row0, ws + WS_KVA + (size_t)(cq * 8) * NT * REC, (size_t)NT * REC, REC, nullptr, lds, ws);
    else proj_tile<2, 0>(xa, w, 1920, nullptr, row0, ws + WS_KVA + (size_t)(cq * 8) * NT * REC, (size_t)NT * REC, REC, nullptr, lds, ws);
  } else {
    char* qb = ws + WS_Q + (size_t)(H + cq * 8) * NT * QREC;
    const u16* w = wq + cq * 160;
    if (cq == 0) proj_tile<0, 1>(xb, w, 640, wk, row0, qb, (size_t)NT * QREC, QREC, ws + WS_KVB, lds, ws);
    else if (cq == 1) proj_tile<0, 2>(xb, w, 640, wv, row0, qb, (size_t)NT * QREC, QREC, ws + WS_KVB, lds, ws);
    else proj_tile<0, 0>(xb, w, 640, nullptr, row0, qb, (size_t)NT * QREC, QREC, nullptr, lds, ws);
  }
}

constexpr int ONST = 5;
constexpr int OSTG = 64 * 128 + WBYTES;
constexpr int OPROJ_LDS = ONST * OSTG;
__global__ __launch_bounds__(512) void oproj_kernel(const u16* __restrict__ o, const u16* __restrict__ woa, const u16* __restrict__ wob, u16* __restrict__ out) {
  extern __shared__ __attribute__((aligned(16))) char lds[];
  PROF_BEGIN(3)
  constexpr int XBYTES = 64 * 128;
  const int tid = threadIdx.x, lane = tid & 63, r = lane & 31, h = lane >> 5;
  const int wid = __builtin_amdgcn_readfirstlane(tid >> 6);
  const int xg = blockIdx.x & 7, j = blockIdx.x >> 3;
  const int br = xg >> 2, rblk = (xg & 3) * 8 + (j & 7), cq = j >> 3;
  const int row0 = rblk * 64;
  const int tt = wid & 1, kh = (wid >> 1) & 1, cg = wid >> 2;
  const unsigned lds0 = (unsigned)(uintptr_t)lds;
  const char* xs = (const char*)(o + ((size_t)br * S + row0) * D);
  const char* w = (const char*)((br ? wob : woa) + cq * 160);
  unsigned xo, wo[3];
  { const int row = 8 * wid + (lane >> 3); xo = row * (D * 2) + (((lane & 7) ^ ((row >> 1) & 7)) << 4); }
  const int wj0 = cg == 0 ? 3 * wid : 12 + 2 * (wid - 4);
#pragma unroll
  for (int i = 0; i < 3; ++i) { const int p = 64 * (wj0 + i) + lane, row = p / 20, c = p - 20 * row; wo[i] = row * (D * 2) + c * 16; }
  auto issue = [&](int c) {
    const unsigned sb = lds0 + (c % ONST) * OSTG;
    const char* xc = xs + c * (BK * 2); const char* wc = w + (size_t)c * BK * D * 2;
    glds16s(xc, xo, sb + wid * 1024);
    glds16s(wc, wo[0], sb + XBYTES + wj0 * 1024); glds16s(wc, wo[1], sb + XBYTES + (wj0 + 1) * 1024);
    if (cg == 0) glds16s(wc, wo[2], sb + XBYTES + (wj0 + 2) * 1024);
  };
  issue(0); issue(1); issue(2); issue(3);
  const unsigned xa = tt * 4096 + r * 128 + ((h ^ ((r >> 1) & 7)) << 4);
  const int g = 4 * ((lane >> 4) & 1) + (lane & 3), q = (lane & 15) >> 2;
  unsigned wa[3];
#pragma unroll
  for (int i = 0; i < 3; ++i) wa[i] = XBYTES + (8 * h + q) * WROW + colmap_oct(cg * 3 + (i < 2 || cg == 0 ? i : 0), g) * 2;
  f32x16 acc[3];
#pragma unroll
  for (int i = 0; i < 3; ++i) acc[i] = f32x16{};
  for (int c = 0; c < NKC; ++c) {
    if (c < NKC - 3) { if (cg == 0) WAITV(12); else WAITV(9); }
    else if (c == NKC - 3) { if (cg == 0) WAITV(8); else WAITV(6); }
    else if (c == NKC - 2) { if (cg == 0) WAITV(4); else WAITV(3); }
    else WAITV(0);
    BARRIER();
    if (c == 0) { PROF_MARK1(3) }
    if (c + 4 < NKC) issue(c + 4);
    const unsigned sb = lds0 + (c % ONST) * OSTG;
#pragma unroll
    for (int k2 = 0; k2 < 2; ++k2) {
      const int ks = 2 * kh + k2;
      const bf16x8 xf = *(const LDSP bf16x8*)(sb + (xa ^ (ks << 5)));
#pragma unroll
      for (int i = 0; i < 3; ++i) {
        if (i == 2 && cg == 1) continue;
        const unsigned a = sb + wa[i] + (16 * ks) * WROW;
        const bf16x4 lo = trread(a), hi = trread(a + 4 * WROW);
        const bf16x8 wf = __builtin_shufflevector(lo, hi, 0, 1, 2, 3, 4, 5, 6, 7);
        acc[i] = MFMA16(wf, xf, acc[i]);
      }
    }
  }
  PROF_MARK2(3)
  BARRIER();
  const unsigned red = lds0 + ((cg * 2 + tt) * 3) * 4096 + lane * 16;
  if (kh == 1) {
#pragma unroll
    for (int i = 0; i < 3; ++i) { if (i == 2 && cg == 1) continue;
#pragma unroll
      for (int qd = 0; qd < 4; ++qd) *(LDSP f32x4*)(red + i * 4096 + qd * 1024) = f32x4{acc[i][4 * qd], acc[i][4 * qd + 1], acc[i][4 * qd + 2], acc[i][4 * qd + 3]}; }
  }
  BARRIER();
  if (kh == 0) {
    u16* op = out + (size_t)(row0 + 32 * tt + r) * 1280 + br * 640 + cq * 160 + 8 * h;
#pragma unroll
    for (int i = 0; i < 3; ++i) { if (i == 2 && cg == 1) continue;
#pragma unroll
      for (int qd = 0; qd < 4; ++qd) { const f32x4 t = *(const LDSP f32x4*)(red + i * 4096 + qd * 1024);
        acc[i][4 * qd] += t[0]; acc[i][4 * qd + 1] += t[1]; acc[i][4 * qd + 2] += t[2]; acc[i][4 * qd + 3] += t[3]; }
#pragma unroll
      for (int o8 = 0; o8 < 2; ++o8) {
        u32x4 v = {cvtpk(acc[i][8 * o8], acc[i][8 * o8 + 1]), cvtpk(acc[i][8 * o8 + 2], acc[i][8 * o8 + 3]), cvtpk(acc[i][8 * o8 + 4], acc[i][8 * o8 + 5]), cvtpk(acc[i][8 * o8 + 6], acc[i][8 * o8 + 7])};
        *(u32x4*)(op + 32 * (cg * 3 + i) + 16 * o8) = v;
      }
    }
  }
  PROF_END(3, (char*)o - WS_O)
}

#define MX3(a, b, c) __builtin_fmaxf(__builtin_fmaxf((a), (b)), (c))
DI float rowmax16(const f32x16& p) {
  float a = MX3(p[0], p[1], p[2]), b = MX3(p[3], p[4], p[5]);
  a = MX3(a, p[6], p[7]); b = MX3(b, p[8], p[9]); a = MX3(a, p[10], p[11]); b = MX3(b, p[12], p[13]); a = MX3(a, p[14], p[15]);
  return __builtin_fmaxf(a, b);
}
DI float xhalf_max(float m) {
  auto rr = __builtin_amdgcn_permlane32_swap(__float_as_uint(m), __float_as_uint(m), false, false);
  return __builtin_fmaxf(__uint_as_float(rr[0]), __uint_as_float(rr[1]));
}
DI bf16x4 negm_frag(float m) {
  const float n = -m;
  const unsigned h1 = cvtpk(n, n) & 0xffffu; const float r1 = n - __uint_as_float(h1 << 16);
  const unsigned h2 = cvtpk(r1, r1) & 0xffffu; const float r2 = r1 - __uint_as_float(h2 << 16);
  const unsigned h3 = cvtpk(r2, r2) & 0xffffu;
  u32x2 p = {h1 | (h2 << 16), h3};
  return __builtin_bit_cast(bf16x4, p);
}

#define KS 1
constexpr int TPC = 8;
constexpr int CTILES = TPC * KS;
constexpr int CH = CTILES * REC;
constexpr int NCH = NT / CTILES;
constexpr int NBUF = KS == 1 ? 4 : 3;
constexpr int AW = 8 * KS;
constexpr int QL_OFF = NBUF * CH + 64;
constexpr int ATT_LDS = QL_OFF + 16 * QREC + (KS == 2 ? 0 : 0);

__global__ __launch_bounds__(64 * AW) void attn_kernel(const char* __restrict__ ws_ro, char* __restrict__ ws) {
  extern __shared__ __attribute__((aligned(16))) char lds[];
  PROF_BEGIN(2)
  const int tid = threadIdx.x, lane = tid & 63, r = lane & 31, h = lane >> 5;
  const int wid = __builtin_amdgcn_readfirstlane(tid >> 6);
  const int wq = wid & 7, kh = wid >> 3;
  const int xg = blockIdx.x & 7, j = blockIdx.x >> 3;
  const int br = j >> 4, head = 4 * xg + ((j >> 2) & 3), rb = j & 3;
  const char* kv = ws_ro + (br ? WS_KVB : WS_KVA + (size_t)head * NT * REC);
  const char* qp = ws_ro + WS_Q + ((size_t)(br * H + head) * NT + rb * 16) * QREC;
  const unsigned lds0 = (unsigned)(uintptr_t)lds;
  volatile LDSP int* flag = (volatile LDSP int*)(lds0 + NBUF * CH);
  const char* dsrc = kv + wid * REC;
  const unsigned ddst = lds0 + wid * REC;
#define DMA_CHUNK(c) do { const char* s_ = dsrc + (size_t)(c) * CH; const unsigned d_ = rfl(ddst + ((c) % NBUF) * CH); \
    glds16s(s_, lane * 16, d_); glds16s(s_ + 1024, lane * 16, d_ + 1024); if (lane < 41) glds16s(s_ + 2048, lane * 16, d_ + 2048); } while (0)

  const int rm = r <= 21 ? r : 21;
  const unsigned ak0 = h * 512 + r * 16, ak1 = h ? 1280 : 1024 + r * 8, av = VOFF + (h * RB + rm) * 16;

  if (KS == 1) { glds16s(qp + wid * 2560, lane * 16, rfl(lds0 + QL_OFF + wid * 2560)); glds16s(qp + wid * 2560 + 1024, lane * 16, rfl(lds0 + QL_OFF + wid * 2560 + 1024));
                 if (lane < 32) glds16s(qp + wid * 2560 + 2048, lane * 16, rfl(lds0 + QL_OFF + wid * 2560 + 2048)); }
  else { glds16s(qp + wid * 1280, lane * 16, rfl(lds0 + QL_OFF + wid * 1280)); if (lane < 16) glds16s(qp + wid * 1280 + 1024, lane * 16, rfl(lds0 + QL_OFF + wid * 1280 + 1024)); }
  if (tid == 0) *flag = 0;
  DMA_CHUNK(0); DMA_CHUNK(1);
  if (NBUF == 4) DMA_CHUNK(2);
  bf16x8 qa0, qb0; bf16x4 qa1, qb1, qa1d, qb1d;
  f32x16 oa, ob;
  for (int attempt = 0; attempt < 2; ++attempt) {
    if (NBUF == 4) asm volatile("s_waitcnt vmcnt(6) lgkmcnt(0)\n\ts_barrier" ::: "memory");
    else asm volatile("s_waitcnt vmcnt(3) lgkmcnt(0)\n\ts_barrier" ::: "memory");
    {
      const unsigned ql = lds0 + QL_OFF + wq * 2560;
      qa0 = *(const LDSP bf16x8*)(ql + h * 512 + r * 16); qa1d = *(const LDSP bf16x4*)(ql + 1024 + r * 8);
      qb0 = *(const LDSP bf16x8*)(ql + 1280 + h * 512 + r * 16); qb1d = *(const LDSP bf16x4*)(ql + 1280 + 1024 + r * 8);
      const bf16x4 z4 = {0, 0, 0, 0};
      const bf16x4 qaz = h ? z4 : qa1d, qbz = h ? z4 : qb1d;
      float ma, mb;
      if (attempt == 0) {
        const bf16x8 kf0 = *(const LDSP bf16x8*)(lds0 + ak0); const bf16x4 kf1 = *(const LDSP bf16x4*)(lds0 + ak1);
        f32x16 s = MFMA16(kf0, qa0, f32x16{}); s = MFMA8(kf1, qaz, s); ma = xhalf_max(rowmax16(s));
        s = MFMA16(kf0, qb0, f32x16{}); s = MFMA8(kf1, qbz, s); mb = xhalf_max(rowmax16(s));
      } else {
        ma = -INFINITY; mb = -INFINITY;
        for (int t = 0; t < NT; ++t) {
          const char* rec = kv + (size_t)t * REC;
          const bf16x8 kf0 = *(const bf16x8*)(rec + ak0); const bf16x4 kf1 = *(const bf16x4*)(rec + ak1);
          f32x16 s = MFMA16(kf0, qa0, f32x16{}); s = MFMA8(kf1, qaz, s); ma = __builtin_fmaxf(ma, rowmax16(s));
          s = MFMA16(kf0, qb0, f32x16{}); s = MFMA8(kf1, qbz, s); mb = __builtin_fmaxf(mb, rowmax16(s));
        }
        ma = xhalf_max(ma); mb = xhalf_max(mb);
      }
      qa1 = h ? negm_frag(ma) : qa1d; qb1 = h ? negm_frag(mb) : qb1d;
    }
    oa = f32x16{}; ob = f32x16{};
    PROF_MARK1(2)
    f32x16 sa, sb; bf16x8 k0, v0, v1; bf16x4 k1; u32x4 pa0, pa1, pb0, pb1;
    const unsigned tb = lds0 + kh * TPC * REC;
    {
      if (NBUF == 4) asm volatile("s_waitcnt vmcnt(3)\n\ts_barrier" ::: "memory");
      else asm volatile("s_waitcnt vmcnt(0)\n\ts_barrier" ::: "memory");
      k0 = *(const LDSP bf16x8*)(tb + ak0); k1 = *(const LDSP bf16x4*)(tb + ak1);
      sa = MFMA16(k0, qa0, f32x16{}); sa = MFMA8(k1, qa1, sa);
      v0 = *(const LDSP bf16x8*)(tb + av); v1 = v0;
      pb0 = u32x4{0, 0, 0, 0}; pb1 = pb0;
    }
#define EX(x) __builtin_amdgcn_exp2f(x)
#define EXP4(SC, i, P, k) do { const float e0 = EX(SC[i]), e1 = EX(SC[i + 1]), e2 = EX(SC[i + 2]), e3 = EX(SC[i + 3]); P[k] = cvtpk(e0, e1); P[k + 1] = cvtpk(e2, e3); PIN(P); SB(); } while (0)
#define BF(x) __builtin_bit_cast(bf16x8, x)
#define STEP(KN0, KN1, VC) do { \
      sb = MFMA16(k0, qb0, f32x16{}); SB(); k0 = *(const LDSP bf16x8*)(KN0);            EXP4(sa, 0, pa0, 0); \
      ob = MFMA16(v0, BF(pb0), ob); SB(); v0 = *(const LDSP bf16x8*)(VC);               EXP4(sa, 4, pa0, 2); \
      sb = MFMA8(k1, qb1, sb); SB(); k1 = *(const LDSP bf16x4*)(KN1);                   EXP4(sa, 8, pa1, 0); \
      ob = MFMA16(v1, BF(pb1), ob); SB(); v1 = *(const LDSP bf16x8*)((VC) + VS);        EXP4(sa, 12, pa1, 2); \
      sa = MFMA16(k0, qa0, f32x16{}); SB();                                             EXP4(sb, 0, pb0, 0); \
      oa = MFMA16(v0, BF(pa0), oa); SB();                                               EXP4(sb, 4, pb0, 2); \
      sa = MFMA8(k1, qa1, sa); SB();                                                    EXP4(sb, 8, pb1, 0); \
      oa = MFMA16(v1, BF(pa1), oa); SB();                                               EXP4(sb, 12, pb1, 2); \
    } while (0)
    for (int c = 0; c < NCH; ++c) {
      if (c > 0) {
        if (NBUF == 4 && c < NCH - 2) asm volatile("s_waitcnt vmcnt(3) lgkmcnt(0)\n\ts_barrier" ::: "memory");
        else asm volatile("s_waitcnt vmcnt(0) lgkmcnt(0)\n\ts_barrier" ::: "memory");
      }
      if (c + NBUF - 1 < NCH) DMA_CHUNK(c + NBUF - 1);
      const unsigned base = tb + (c % NBUF) * CH;
      const unsigned basen = tb + ((c + 1) % NBUF) * CH;
      const LDSP char* pk0 = (const LDSP char*)(base + ak0);
      const LDSP char* pk1 = (const LDSP char*)(base + ak1);
      const LDSP char* pv = (const LDSP char*)(base + av);
      const LDSP char* nk0 = (const LDSP char*)(basen + ak0);
      const LDSP char* nk1 = (const LDSP char*)(basen + ak1);
#pragma unroll
      for (int t = 0; t < TPC; ++t)
        STEP(t + 1 < TPC ? pk0 + (t + 1) * REC : nk0, t + 1 < TPC ? pk1 + (t + 1) * REC : nk1, pv + t * REC);
    }
    ob = MFMA16(v0, BF(pb0), ob); ob = MFMA16(v1, BF(pb1), ob);
    PROF_MARK2(2)
    if (KS == 2) {
      asm volatile("s_waitcnt vmcnt(0) lgkmcnt(0)\n\ts_barrier" ::: "memory");
      const unsigned red = lds0 + wq * 8192 + lane * 16;
      if (kh == 1) {
#pragma unroll
        for (int qd = 0; qd < 3; ++qd) { *(LDSP f32x4*)(red + qd * 1024) = f32x4{oa[4 * qd], oa[4 * qd + 1], oa[4 * qd + 2], oa[4 * qd + 3]};
                                         *(LDSP f32x4*)(red + 4096 + qd * 1024) = f32x4{ob[4 * qd], ob[4 * qd + 1], ob[4 * qd + 2], ob[4 * qd + 3]}; }
      }
      asm volatile("s_waitcnt lgkmcnt(0)\n\ts_barrier" ::: "memory");
      if (kh == 0) {
#pragma unroll
        for (int qd = 0; qd < 3; ++qd) { const f32x4 ta = *(const LDSP f32x4*)(red + qd * 1024), tb2 = *(const LDSP f32x4*)(red + 4096 + qd * 1024);
#pragma unroll
          for (int e = 0; e < 4; ++e) { oa[4 * qd + e] += ta[e]; ob[4 * qd + e] += tb2[e]; } }
      }
    }
    bool bad = false;
    if (kh == 0) {
#pragma unroll
      for (int qb = 0; qb < 2; ++qb) {
        const f32x16& o = qb ? ob : oa;
        auto rr = __builtin_amdgcn_permlane32_swap(__float_as_uint(o[8]), __float_as_uint(o[8]), false, false);
        const float l = __uint_as_float(rr[1]);
        bad |= !(l <= 1.099511627776e12f);
        const float inv = 1.0f / l;
        u16* op = (u16*)(ws + WS_O) + ((size_t)br * S + rb * 512 + wq * 64 + qb * 32 + r) * D + head * DH;
        *(u32x2*)(op + 4 * h) = u32x2{cvtpk(o[0] * inv, o[1] * inv), cvtpk(o[2] * inv, o[3] * inv)};
        *(u32x2*)(op + 8 + 4 * h) = u32x2{cvtpk(o[4] * inv, o[5] * inv), cvtpk(o[6] * inv, o[7] * inv)};
        if (h == 0) *(u32x2*)(op + 16) = u32x2{cvtpk(o[8] * inv, o[9] * inv), cvtpk(o[10] * inv, o[11] * inv)};
      }
    }
    if (attempt == 1) break;
    if (__any(bad)) *flag = 1;
    asm volatile("s_waitcnt lgkmcnt(0)\n\ts_barrier" ::: "memory");
    if (*flag == 0) break;
    asm volatile("s_waitcnt vmcnt(0) lgkmcnt(0)\n\ts_barrier" ::: "memory");
    DMA_CHUNK(0); DMA_CHUNK(1);
    if (NBUF == 4) DMA_CHUNK(2);
  }
  PROF_END(2, ws)
#undef DMA_CHUNK
}

extern "C" void kernel_launch(void* const* d_in, const int* in_sizes, int n_in, void* d_out, int out_size, void* d_ws, size_t ws_size, hipStream_t stream) {
  const u16* xa = (const u16*)d_in[0]; const u16* wqkv = (const u16*)d_in[1]; const u16* woa = (const u16*)d_in[2];
  const u16* xb = (const u16*)d_in[3]; const u16* wq = (const u16*)d_in[4]; const u16* wk = (const u16*)d_in[5];
  const u16* wv = (const u16*)d_in[6]; const u16* wob = (const u16*)d_in[7];
  char* ws = (char*)d_ws;
  (void)hipFuncSetAttribute((const void*)proj_kernel, hipFuncAttributeMaxDynamicSharedMemorySize, PROJ_LDS);
  (void)hipFuncSetAttribute((const void*)attn_kernel, hipFuncAttributeMaxDynamicSharedMemorySize, ATT_LDS);
  (void)hipFuncSetAttribute((const void*)oproj_kernel, hipFuncAttributeMaxDynamicSharedMemorySize, OPROJ_LDS);
  proj_kernel<<<256, 512, PROJ_LDS, stream>>>(xa, wqkv, xb, wq, wk, wv, ws);
  attn_kernel<<<256, 64 * AW, ATT_LDS, stream>>>(ws, ws);
  oproj_kernel<<<256, 512, OPROJ_LDS, stream>>>((const u16*)(ws + WS_O), woa, wob, (u16*)d_out);
}
```

```cpp
#include <hip/hip_runtime.h>
#include <hip/hip_bf16.h>
#include <stdint.h>

typedef unsigned short u16;
using bf16x8 = __attribute__((ext_vector_type(8))) short;
using bf16x4 = __attribute__((ext_vector_type(4))) short;
using f32x16 = __attribute__((ext_vector_type(16))) float;
using u32x4 = __attribute__((ext_vector_type(4))) unsigned;
using u32x2 = __attribute__((ext_vector_type(2))) unsigned;
using f32x4 = __attribute__((ext_vector_type(4))) float;
typedef float f32x2 __attribute__((ext_vector_type(2)));
typedef __bf16 bfv2 __attribute__((ext_vector_type(2)));
typedef short v4i16_t __attribute__((ext_vector_type(4)));
#define DI __device__ __forceinline__
#define LDSP __attribute__((address_space(3)))

constexpr int S = 2048, D = 640, H = 32, DH = 20;
constexpr int NT = S / 32;
constexpr int RB = 22;
constexpr int VOFF = 1296;
constexpr int VS = 2 * RB * 16;
constexpr int REC = VOFF + 4 * RB * 16;
constexpr int QREC = 1280;
constexpr size_t WS_KVA = 0;
constexpr size_t WS_KVB = WS_KVA + (size_t)H * NT * REC;
constexpr size_t WS_Q = WS_KVB + (size_t)NT * REC + 4096;
constexpr size_t WS_O = WS_Q + (size_t)2 * H * NT * QREC + 4096;
constexpr size_t WS_END = WS_O + (size_t)2 * S * D * 2;
constexpr float QSCALE = 0.22360679774997896f * 1.4426950408889634f;

DI unsigned cvtpk(float lo, float hi) { f32x2 v = {lo, hi}; bfv2 b = __builtin_convertvector(v, bfv2); return __builtin_bit_cast(unsigned, b); }
DI void glds16(const void* g, unsigned lds_base) {
  unsigned sv;
  asm volatile("s_mov_b32 %0, m0\n\ts_mov_b32 m0, %2\n\ts_nop 0\n\tglobal_load_lds_dwordx4 %1, off\n\ts_mov_b32 m0, %0" : "=&s"(sv) : "v"(g), "s"(lds_base) : "memory");
}
DI void glds16s(const void* sbase, unsigned voff, unsigned lds_base) {
  unsigned sv;
  asm volatile("s_mov_b32 %0, m0\n\ts_mov_b32 m0, %3\n\ts_nop 0\n\tglobal_load_lds_dwordx4 %1, %2\n\ts_mov_b32 m0, %0" : "=&s"(sv) : "v"(voff), "s"(sbase), "s"(lds_base) : "memory");
}
#define MFMA16(a, b, c) __builtin_amdgcn_mfma_f32_32x32x16_bf16(a, b, c, 0, 0, 0)
#define MFMA8(a, b, c) __builtin_amdgcn_mfma_f32_32x32x8bf16_1k(a, b, c, 0, 0, 0)
#define SB() __builtin_amdgcn_sched_barrier(0)
#define PIN(x) asm volatile("" : "+v"(x))
DI unsigned rfl(unsigned x) { return (unsigned)__builtin_amdgcn_readfirstlane((int)x); }
DI bf16x4 trread(unsigned a) { return __builtin_bit_cast(bf16x4, __builtin_amdgcn_ds_read_tr16_b64_v4i16((LDSP v4i16_t*)a)); }

constexpr int BK = 64, NKC = D / BK, NST = 4;
constexpr int WROW = 320;
constexpr int WBYTES = BK * WROW;
constexpr int EBYTES = 3072;
DI int colmap_head(int ct, int g) {
  const int o = g >> 2, hi = g & 1, u = (g >> 1) & 1;
  return ct < 4 ? 20 * (ct + 4 * o) + 8 * hi + 4 * u : 20 * (4 * o + 2 * hi + u) + 16;
}
DI int colmap_oct(int ct, int g) { return 32 * ct + 16 * (g >> 2) + 8 * (g & 1) + 4 * ((g >> 1) & 1); }
DI int colmap_id(int ct, int g) { return 32 * ct + 4 * g; }

#define PROF_BEGIN(k)
#define PROF_MARK1(k)
#define PROF_MARK2(k)
#define PROF_END(k, wsp)
#define WAITV(n) asm volatile("s_waitcnt vmcnt(" #n ")" ::: "memory")
#define BARRIER() asm volatile("s_waitcnt lgkmcnt(0)\n\ts_barrier" ::: "memory")

template <int NK, int NA, int NSTG, int STG, bool V04, bool V5, bool ODD, class Mark>
DI void gemm_consume(f32x16 (&acc)[6], const unsigned lds0, const unsigned xa, const unsigned (&wa)[4], const int ks0, Mark&& mark) {
  bf16x8 xf[2][2], wf[2][4];
#define RDX(set, sb, ks) do { xf[set][0] = *(const LDSP bf16x8*)((sb) + (xa ^ ((ks) << 5))); xf[set][1] = *(const LDSP bf16x8*)((sb) + 4096 + (xa ^ ((ks) << 5))); } while (0)
#define RDW(set, sb, ks, i) do { const unsigned rs_ = (i) == 3 ? 40 : WROW; const unsigned a_ = (sb) + wa[i] + (16 * (ks)) * rs_; \
      const bf16x4 lo_ = trread(a_), hi_ = trread(a_ + 4 * rs_); wf[set][i] = __builtin_shufflevector(lo_, hi_, 0, 1, 2, 3, 4, 5, 6, 7); } while (0)
#define MM1(i, X, W, V) acc[i] = (V) ? MFMA16(X, W, acc[i]) : MFMA16(W, X, acc[i])
#define STEPG(cur, sb, ks) do { \
    MM1(0, xf[cur][0], wf[cur][0], V04); SB(); RDX((cur) ^ 1, sb, ks); SB(); \
    MM1(1, xf[cur][1], wf[cur][0], V04); SB(); RDW((cur) ^ 1, sb, ks, 0); SB(); \
    MM1(2, xf[cur][0], wf[cur][1], V04); SB(); RDW((cur) ^ 1, sb, ks, 1); SB(); \
    MM1(3, xf[cur][1], wf[cur][1], V04); SB(); RDW((cur) ^ 1, sb, ks, 2); SB(); \
    MM1(4, xf[cur][ODD ? 1 : 0], wf[cur][2], V04); SB(); if (NA == 6) { RDW((cur) ^ 1, sb, ks, 3); SB(); MM1(5, xf[cur][ODD ? 1 : 0], wf[cur][3], V5); SB(); } } while (0)
  asm volatile("s_barrier" ::: "memory");
  mark();
  RDX(0, lds0, ks0); RDW(0, lds0, ks0, 0); RDW(0, lds0, ks0, 1); RDW(0, lds0, ks0, 2); if (NA == 6) RDW(0, lds0, ks0, 3);
  unsigned sb = lds0; int slot = 0;
#pragma unroll 2
  for (int c = 0; c < NKC; ++c) {
#pragma unroll
    for (int k = 0; k + 1 < NK; ++k) STEPG(k & 1, sb, ks0 + k + 1);
    asm volatile("s_waitcnt lgkmcnt(0)\n\ts_barrier" ::: "memory");
    slot = slot + 1 == NSTG ? 0 : slot + 1; sb = lds0 + slot * STG;
    STEPG(1, sb, ks0);
  }
#undef RDX
#undef RDW
#undef MM1
#undef STEPG
}
template <int NSTG, int STG, int NI, class Issue>
DI void gemm_load(const unsigned lds0, Issue&& issue) {
#pragma unroll
  for (int c = 0; c < NSTG; ++c) issue(c, lds0 + c * STG);
  asm volatile("s_waitcnt vmcnt(%0)\n\ts_barrier" :: "n"((NSTG - 1) * NI) : "memory");
  unsigned sb = lds0; int slot = 0;
  for (int c = 0; c < NKC; ++c) {
    const int rem = NKC - 2 - c;
    if (rem >= NSTG - 2) asm volatile("s_waitcnt vmcnt(%0)\n\ts_barrier" :: "n"((NSTG - 2) * NI) : "memory");
    else if (NSTG > 3 && rem == NSTG - 3) asm volatile("s_waitcnt vmcnt(%0)\n\ts_barrier" :: "n"((NSTG - 3) * NI) : "memory");
    else if (NSTG > 4 && rem == NSTG - 4) asm volatile("s_waitcnt vmcnt(%0)\n\ts_barrier" :: "n"(NSTG > 4 ? (NSTG - 4) * NI : 0) : "memory");
    else asm volatile("s_waitcnt vmcnt(0)\n\ts_barrier" ::: "memory");
    if (c + NSTG < NKC) issue(c + NSTG, sb);
    slot = slot + 1 == NSTG ? 0 : slot + 1; sb = lds0 + slot * STG;
  }
}

template <int KIND, int EXTRA>
DI void proj_tile(const u16* __restrict__ x, const u16* __restrict__ w, int ldw, const u16* __restrict__ we, int row0,
                  char* qk_base  , size_t head_stride, int rec, char* e_base, char* lds, char* wsp) {
  PROF_BEGIN(1)
  constexpr int XBYTES = 128 * 128;
  constexpr int STG = XBYTES + WBYTES + EBYTES;
  const int tid = threadIdx.x, lane = tid & 63, r = lane & 31, h = lane >> 5;
  const int wid = __builtin_amdgcn_readfirstlane(tid >> 6);
  const unsigned lds0 = (unsigned)(uintptr_t)lds;
  if (wid >= 4) {
    const int lw = wid - 4;
    unsigned xo[4], wo[5];
#pragma unroll
    for (int i = 0; i < 4; ++i) { const int row = 8 * (4 * lw + i) + (lane >> 3); xo[i] = row * (D * 2) + (((lane & 7) ^ ((row >> 1) & 7)) << 4); }
#pragma unroll
    for (int i = 0; i < 5; ++i) { const int p = 64 * (5 * lw + i) + lane, row = p / 20, c = p - 20 * row; wo[i] = row * ldw * 2 + c * 16; }
    const char* xs = (const char*)x + (size_t)row0 * D * 2;
    const int ep = lw < 2 ? lw : 2;
    const unsigned eo = ep * 1024 + (ep == 2 ? (lane & 31) : lane) * 16;
    auto issue = [&](int c, unsigned sb) {
      const char* xc = xs + c * (BK * 2); const char* wc = (const char*)w + (size_t)c * BK * ldw * 2;
#pragma unroll
      for (int i = 0; i < 4; ++i) glds16s(xc, xo[i], sb + (4 * lw + i) * 1024);
#pragma unroll
      for (int i = 0; i < 5; ++i) glds16s(wc, wo[i], sb + XBYTES + (5 * lw + i) * 1024);
      if (EXTRA != 0) glds16s((const char*)we + c * 2560, eo, sb + XBYTES + WBYTES + ep * 1024);
    };
    gemm_load<NST, STG, EXTRA != 0 ? 10 : 9>(lds0, issue);
    PROF_END(1, wsp)
    return;
  }
  const int tp = wid >> 1, odd = wid & 1;
  const unsigned xa = tp * 8192 + r * 128 + ((h ^ ((r >> 1) & 7)) << 4);
  const int g = 4 * ((lane >> 4) & 1) + (lane & 3), q = (lane & 15) >> 2;
  unsigned wa[4];
#pragma unroll
  for (int i = 0; i < 3; ++i) { const int ct = i < 2 ? 2 * odd + i : 4; wa[i] = XBYTES + (8 * h + q) * WROW + (KIND == 2 ? colmap_id(ct, g) : colmap_head(ct, g)) * 2; }
  { int col;
    if (EXTRA == 1) { const int w8 = 2 * (g >> 2) + (g & 1), u = (g >> 1) & 1; col = w8 < 2 ? 8 * w8 + 4 * u : (w8 == 2 && u == 0 ? 16 : 0); }
    else col = g < 5 ? 4 * g : 0;
    wa[3] = XBYTES + WBYTES + (8 * h + q) * 40 + col * 2; }
  constexpr int NA = EXTRA != 0 ? 6 : 5;
  f32x16 acc[6];
#pragma unroll
  for (int i = 0; i < 6; ++i) acc[i] = f32x16{};
  auto mark = [&]() { PROF_MARK1(1) };
  if (odd) gemm_consume<4, NA, NST, STG, KIND == 2, EXTRA == 2, true>(acc, lds0, xa, wa, 0, mark);
  else gemm_consume<4, NA, NST, STG, KIND == 2, EXTRA == 2, false>(acc, lds0, xa, wa, 0, mark);
  PROF_MARK2(1)
#pragma unroll
  for (int i = 0; i < NA; ++i) {
    const bool ex = i == 5;
    const int ct = i < 4 ? 2 * odd + (i >> 1) : 4;
    const int tile = (row0 >> 5) + (i < 4 ? 2 * tp + (i & 1) : wid);
    if (ex ? (EXTRA == 2) : (KIND == 2)) {
      const int c = ex ? r : 32 * ct + r;
      const int hl = ex ? 0 : c / 20, d = ex ? r : c - 20 * hl;
      if (!ex || r < 20) {
        char* rb = (ex ? e_base : qk_base + hl * head_stride) + (size_t)tile * REC + VOFF + (h * RB) * 16;
#pragma unroll
        for (int s = 0; s < 2; ++s) {
          u32x4 v = {cvtpk(acc[i][8 * s], acc[i][8 * s + 1]), cvtpk(acc[i][8 * s + 2], acc[i][8 * s + 3]), cvtpk(acc[i][8 * s + 4], acc[i][8 * s + 5]), cvtpk(acc[i][8 * s + 6], acc[i][8 * s + 7])};
          *(u32x4*)(rb + s * VS + d * 16) = v;
          if (d == 0) {
            *(u32x4*)(rb + s * VS + 20 * 16) = u32x4{0x3F803F80u, 0x3F803F80u, 0x3F803F80u, 0x3F803F80u};
            *(u32x4*)(rb + s * VS + 21 * 16) = u32x4{0u, 0u, 0u, 0u};
          }
        }
      }
    } else {
      const bool isq = !ex && KIND == 0;
      const float sc = isq ? QSCALE : 1.0f;
#pragma unroll
      for (int o = 0; o < 2; ++o) {
        u32x4 v = {cvtpk(acc[i][8 * o] * sc, acc[i][8 * o + 1] * sc), cvtpk(acc[i][8 * o + 2] * sc, acc[i][8 * o + 3] * sc),
                   cvtpk(acc[i][8 * o + 4] * sc, acc[i][8 * o + 5] * sc), cvtpk(acc[i][8 * o + 6] * sc, acc[i][8 * o + 7] * sc)};
        if (ex) {
          char* rb = e_base + (size_t)tile * REC;
          if (o == 0) *(u32x4*)(rb + h * 512 + r * 16) = v;
          else if (h == 0) { *(u32x2*)(rb + 1024 + r * 8) = u32x2{v[0], v[1]}; if (r == 0) *(u32x2*)(rb + 1280) = u32x2{0x3F803F80u, 0x00003F80u}; }
        } else if (i < 4) {
          *(u32x4*)(qk_base + (size_t)(ct + 4 * o) * head_stride + (size_t)tile * rec + h * 512 + r * 16) = v;
        } else {
          char* rb = qk_base + (size_t)(4 * o + 2 * h) * head_stride + (size_t)tile * rec;
          *(u32x2*)(rb + 1024 + r * 8) = u32x2{v[0], v[1]};
          *(u32x2*)(rb + head_stride + 1024 + r * 8) = u32x2{v[2], v[3]};
          if (KIND == 1 && r == 0) { *(u32x2*)(rb + 1280) = u32x2{0x3F803F80u, 0x00003F80u}; *(u32x2*)(rb + head_stride + 1280) = u32x2{0x3F803F80u, 0x00003F80u}; }
        }
      }
    }
  }
  PROF_END(1, wsp)
}
constexpr int PROJ_LDS = NST * (128 * 128 + WBYTES + EBYTES);

__global__ __launch_bounds__(512) void proj_kernel(const u16* __restrict__ xa, const u16* __restrict__ wqkv, const u16* __restrict__ xb, const u16* __restrict__ wq,
                                                   const u16* __restrict__ wk, const u16* __restrict__ wv, char* __restrict__ ws) {
  extern __shared__ __attribute__((aligned(16))) char lds[];
  const int xg = blockIdx.x & 7, j = blockIdx.x >> 3;
  const int rblk = (xg & 1) * 8 + (j & 7), cq = j >> 3;
  const int row0 = rblk * 128;
  if (xg < 6) {
    const int kind = xg >> 1;
    const u16* w = wqkv + kind * 640 + cq * 160;
    if (kind == 0) proj_tile<0, 0>(xa, w, 1920, nullptr, row0, ws + WS_Q + (size_t)(cq * 8) * NT * QREC, (size_t)NT * QREC, QREC, nullptr, lds, ws);
    else if (kind == 1) proj_tile<1, 0>(xa, w, 1920, nullptr, row0, ws + WS_KVA + (size_t)(cq * 8) * NT * REC, (size_t)NT * REC, REC, nullptr, lds, ws);
    else proj_tile<2, 0>(xa, w, 1920, nullptr, row0, ws + WS_KVA + (size_t)(cq * 8) * NT * REC, (size_t)NT * REC, REC, nullptr, lds, ws);
  } else {
    char* qb = ws + WS_Q + (size_t)(H + cq * 8) * NT * QREC;
    const u16* w = wq + cq * 160;
    if (cq == 0) proj_tile<0, 1>(xb, w, 640, wk, row0, qb, (size_t)NT * QREC, QREC, ws + WS_KVB, lds, ws);
    else if (cq == 1) proj_tile<0, 2>(xb, w, 640, wv, row0, qb, (size_t)NT * QREC, QREC, ws + WS_KVB, lds, ws);
    else proj_tile<0, 0>(xb, w, 640, nullptr, row0, qb, (size_t)NT * QREC, QREC, nullptr, lds, ws);
  }
}

constexpr int ONST = 5;
constexpr int OSTG = 64 * 128 + WBYTES;
constexpr int OPROJ_LDS = ONST * OSTG;
__global__ __launch_bounds__(512) void oproj_kernel(const u16* __restrict__ o, const u16* __restrict__ woa, const u16* __restrict__ wob, u16* __restrict__ out) {
  extern __shared__ __attribute__((aligned(16))) char lds[];
  PROF_BEGIN(3)
  constexpr int XBYTES = 64 * 128;
  const int tid = threadIdx.x, lane = tid & 63, r = lane & 31, h = lane >> 5;
  const int wid = __builtin_amdgcn_readfirstlane(tid >> 6);
  const int xg = blockIdx.x & 7, j = blockIdx.x >> 3;
  const int br = xg >> 2, rblk = (xg & 3) * 8 + (j & 7), cq = j >> 3;
  const int row0 = rblk * 64;
  const unsigned lds0 = (unsigned)(uintptr_t)lds;
  if (wid >= 4) {
    const int lw = wid - 4;
    const char* xs = (const char*)(o + ((size_t)br * S + row0) * D);
    const char* w = (const char*)((br ? wob : woa) + cq * 160);
    unsigned xo[2], wo[5];
#pragma unroll
    for (int i = 0; i < 2; ++i) { const int row = 8 * (2 * lw + i) + (lane >> 3); xo[i] = row * (D * 2) + (((lane & 7) ^ ((row >> 1) & 7)) << 4); }
#pragma unroll
    for (int i = 0; i < 5; ++i) { const int p = 64 * (5 * lw + i) + lane, row = p / 20, c = p - 20 * row; wo[i] = row * (D * 2) + c * 16; }
    auto issue = [&](int c, unsigned sb) {
      const char* xc = xs + c * (BK * 2); const char* wc = w + (size_t)c * BK * D * 2;
#pragma unroll
      for (int i = 0; i < 2; ++i) glds16s(xc, xo[i], sb + (2 * lw + i) * 1024);
#pragma unroll
      for (int i = 0; i < 5; ++i) glds16s(wc, wo[i], sb + XBYTES + (5 * lw + i) * 1024);
    };
    gemm_load<ONST, OSTG, 7>(lds0, issue);
    asm volatile("s_barrier\n\ts_barrier" ::: "memory");
    PROF_END(3, (char*)o - WS_O)
    return;
  }
  const int odd = wid & 1, kh = wid >> 1;
  const unsigned xa = r * 128 + ((h ^ ((r >> 1) & 7)) << 4);
  const int g = 4 * ((lane >> 4) & 1) + (lane & 3), q = (lane & 15) >> 2;
  unsigned wa[4];
#pragma unroll
  for (int i = 0; i < 3; ++i) wa[i] = XBYTES + (8 * h + q) * WROW + colmap_oct(i < 2 ? 2 * odd + i : 4, g) * 2;
  wa[3] = 0;
  f32x16 acc[6];
#pragma unroll
  for (int i = 0; i < 6; ++i) acc[i] = f32x16{};
  auto mark = [&]() { PROF_MARK1(3) };
  if (odd) gemm_consume<2, 5, ONST, OSTG, false, false, true>(acc, lds0, xa, wa, 2 * kh, mark);
  else gemm_consume<2, 5, ONST, OSTG, false, false, false>(acc, lds0, xa, wa, 2 * kh, mark);
  PROF_MARK2(3)
  BARRIER();
  const unsigned red = lds0 + odd * (5 * 4096) + lane * 16;
  if (kh == 1) {
#pragma unroll
    for (int i = 0; i < 5; ++i)
#pragma unroll
      for (int qd = 0; qd < 4; ++qd) *(LDSP f32x4*)(red + i * 4096 + qd * 1024) = f32x4{acc[i][4 * qd], acc[i][4 * qd + 1], acc[i][4 * qd + 2], acc[i][4 * qd + 3]};
  }
  BARRIER();
  if (kh == 0) {
#pragma unroll
    for (int i = 0; i < 5; ++i) {
      const int ct = i < 4 ? 2 * odd + (i >> 1) : 4, tt = i < 4 ? (i & 1) : odd;
      u16* op = out + (size_t)(row0 + 32 * tt + r) * 1280 + br * 640 + cq * 160 + 32 * ct + 8 * h;
#pragma unroll
      for (int qd = 0; qd < 4; ++qd) { const f32x4 t = *(const LDSP f32x4*)(red + i * 4096 + qd * 1024);
        acc[i][4 * qd] += t[0]; acc[i][4 * qd + 1] += t[1]; acc[i][4 * qd + 2] += t[2]; acc[i][4 * qd + 3] += t[3]; }
#pragma unroll
      for (int o8 = 0; o8 < 2; ++o8) {
        u32x4 v = {cvtpk(acc[i][8 * o8], acc[i][8 * o8 + 1]), cvtpk(acc[i][8 * o8 + 2], acc[i][8 * o8 + 3]), cvtpk(acc[i][8 * o8 + 4], acc[i][8 * o8 + 5]), cvtpk(acc[i][8 * o8 + 6], acc[i][8 * o8 + 7])};
        *(u32x4*)(op + 16 * o8) = v;
      }
    }
  }
  PROF_END(3, (char*)o - WS_O)
}

#define MX3(a, b, c) __builtin_fmaxf(__builtin_fmaxf((a), (b)), (c))
DI float rowmax16(const f32x16& p) {
  float a = MX3(p[0], p[1], p[2]), b = MX3(p[3], p[4], p[5]);
  a = MX3(a, p[6], p[7]); b = MX3(b, p[8], p[9]); a = MX3(a, p[10], p[11]); b = MX3(b, p[12], p[13]); a = MX3(a, p[14], p[15]);
  return __builtin_fmaxf(a, b);
}
DI float xhalf_max(float m) {
  auto rr = __builtin_amdgcn_permlane32_swap(__float_as_uint(m), __float_as_uint(m), false, false);
  return __builtin_fmaxf(__uint_as_float(rr[0]), __uint_as_float(rr[1]));
}
DI bf16x4 negm_frag(float m) {
  const float n = -m;
  const unsigned h1 = cvtpk(n, n) & 0xffffu; const float r1 = n - __uint_as_float(h1 << 16);
  const unsigned h2 = cvtpk(r1, r1) & 0xffffu; const float r2 = r1 - __uint_as_float(h2 << 16);
  const unsigned h3 = cvtpk(r2, r2) & 0xffffu;
  u32x2 p = {h1 | (h2 << 16), h3};
  return __builtin_bit_cast(bf16x4, p);
}

#define KS 1
constexpr int TPC = 8;
constexpr int CTILES = TPC * KS;
constexpr int CH = CTILES * REC;
constexpr int NCH = NT / CTILES;
constexpr int NBUF = KS == 1 ? 4 : 3;
constexpr int AW = 8 * KS;
constexpr int QL_OFF = NBUF * CH + 64;
constexpr int ATT_LDS = QL_OFF + 16 * QREC + (KS == 2 ? 0 : 0);

__global__ __launch_bounds__(64 * AW) void attn_kernel(const char* __restrict__ ws_ro, char* __restrict__ ws) {
  extern __shared__ __attribute__((aligned(16))) char lds[];
  PROF_BEGIN(2)
  const int tid = threadIdx.x, lane = tid & 63, r = lane & 31, h = lane >> 5;
  const int wid = __builtin_amdgcn_readfirstlane(tid >> 6);
  const int wq = wid & 7, kh = wid >> 3;
  const int xg = blockIdx.x & 7, j = blockIdx.x >> 3;
  const int br = j >> 4, head = 4 * xg + ((j >> 2) & 3), rb = j & 3;
  const char* kv = ws_ro + (br ? WS_KVB : WS_KVA + (size_t)head * NT * REC);
  const char* qp = ws_ro + WS_Q + ((size_t)(br * H + head) * NT + rb * 16) * QREC;
  const unsigned lds0 = (unsigned)(uintptr_t)lds;
  volatile LDSP int* flag = (volatile LDSP int*)(lds0 + NBUF * CH);
  const char* dsrc = kv + wid * REC;
  const unsigned ddst = lds0 + wid * REC;
#define DMA_CHUNK(c) do { const char* s_ = dsrc + (size_t)(c) * CH; const unsigned d_ = rfl(ddst + ((c) % NBUF) * CH); \
    glds16s(s_, lane * 16, d_); glds16s(s_ + 1024, lane * 16, d_ + 1024); if (lane < 41) glds16s(s_ + 2048, lane * 16, d_ + 2048); } while (0)

  const int rm = r <= 21 ? r : 21;
  const unsigned ak0 = h * 512 + r * 16, ak1 = h ? 1280 : 1024 + r * 8, av = VOFF + (h * RB + rm) * 16;

  if (KS == 1) { glds16s(qp + wid * 2560, lane * 16, rfl(lds0 + QL_OFF + wid * 2560)); glds16s(qp + wid * 2560 + 1024, lane * 16, rfl(lds0 + QL_OFF + wid * 2560 + 1024));
                 if (lane < 32) glds16s(qp + wid * 2560 + 2048, lane * 16, rfl(lds0 + QL_OFF + wid * 2560 + 2048)); }
  else { glds16s(qp + wid * 1280, lane * 16, rfl(lds0 + QL_OFF + wid * 1280)); if (lane < 16) glds16s(qp + wid * 1280 + 1024, lane * 16, rfl(lds0 + QL_OFF + wid * 1280 + 1024)); }
  if (tid == 0) *flag = 0;
  DMA_CHUNK(0); DMA_CHUNK(1);
  if (NBUF == 4) DMA_CHUNK(2);
  bf16x8 qa0, qb0; bf16x4 qa1, qb1, qa1d, qb1d;
  f32x16 oa, ob;
  for (int attempt = 0; attempt < 2; ++attempt) {
    if (NBUF == 4) asm volatile("s_waitcnt vmcnt(6) lgkmcnt(0)\n\ts_barrier" ::: "memory");
    else asm volatile("s_waitcnt vmcnt(3) lgkmcnt(0)\n\ts_barrier" ::: "memory");
    {
      const unsigned ql = lds0 + QL_OFF + wq * 2560;
      qa0 = *(const LDSP bf16x8*)(ql + h * 512 + r * 16); qa1d = *(const LDSP bf16x4*)(ql + 1024 + r * 8);
      qb0 = *(const LDSP bf16x8*)(ql + 1280 + h * 512 + r * 16); qb1d = *(const LDSP bf16x4*)(ql + 1280 + 1024 + r * 8);
      const bf16x4 z4 = {0, 0, 0, 0};
      const bf16x4 qaz = h ? z4 : qa1d, qbz = h ? z4 : qb1d;
      float ma, mb;
      if (attempt == 0) {
        const bf16x8 kf0 = *(const LDSP bf16x8*)(lds0 + ak0); const bf16x4 kf1 = *(const LDSP bf16x4*)(lds0 + ak1);
        f32x16 s = MFMA16(kf0, qa0, f32x16{}); s = MFMA8(kf1, qaz, s); ma = xhalf_max(rowmax16(s));
        s = MFMA16(kf0, qb0, f32x16{}); s = MFMA8(kf1, qbz, s); mb = xhalf_max(rowmax16(s));
      } else {
        ma = -INFINITY; mb = -INFINITY;
        for (int t = 0; t < NT; ++t) {
          const char* rec = kv + (size_t)t * REC;
          const bf16x8 kf0 = *(const bf16x8*)(rec + ak0); const bf16x4 kf1 = *(const bf16x4*)(rec + ak1);
          f32x16 s = MFMA16(kf0, qa0, f32x16{}); s = MFMA8(kf1, qaz, s); ma = __builtin_fmaxf(ma, rowmax16(s));
          s = MFMA16(kf0, qb0, f32x16{}); s = MFMA8(kf1, qbz, s); mb = __builtin_fmaxf(mb, rowmax16(s));
        }
        ma = xhalf_max(ma); mb = xhalf_max(mb);
      }
      qa1 = h ? negm_frag(ma) : qa1d; qb1 = h ? negm_frag(mb) : qb1d;
    }
    oa = f32x16{}; ob = f32x16{};
    PROF_MARK1(2)
    f32x16 sa, sb; bf16x8 k0, v0, v1; bf16x4 k1; u32x4 pa0, pa1, pb0, pb1;
    const unsigned tb = lds0 + kh * TPC * REC;
    {
      if (NBUF == 4) asm volatile("s_waitcnt vmcnt(3)\n\ts_barrier" ::: "memory");
      else asm volatile("s_waitcnt vmcnt(0)\n\ts_barrier" ::: "memory");
      k0 = *(const LDSP bf16x8*)(tb + ak0); k1 = *(const LDSP bf16x4*)(tb + ak1);
      sa = MFMA16(k0, qa0, f32x16{}); sa = MFMA8(k1, qa1, sa);
      v0 = *(const LDSP bf16x8*)(tb + av); v1 = v0;
      pb0 = u32x4{0, 0, 0, 0}; pb1 = pb0;
    }
#define EX(x) __builtin_amdgcn_exp2f(x)
#define EXP4(SC, i, P, k) do { const float e0 = EX(SC[i]), e1 = EX(SC[i + 1]), e2 = EX(SC[i + 2]), e3 = EX(SC[i + 3]); P[k] = cvtpk(e0, e1); P[k + 1] = cvtpk(e2, e3); PIN(P); SB(); } while (0)
#define BF(x) __builtin_bit_cast(bf16x8, x)
#define STEP(KN0, KN1, VC) do { \
      sb = MFMA16(k0, qb0, f32x16{}); SB(); k0 = *(const LDSP bf16x8*)(KN0);            EXP4(sa, 0, pa0, 0); \
      ob = MFMA16(v0, BF(pb0), ob); SB(); v0 = *(const LDSP bf16x8*)(VC);               EXP4(sa, 4, pa0, 2); \
      sb = MFMA8(k1, qb1, sb); SB(); k1 = *(const LDSP bf16x4*)(KN1);                   EXP4(sa, 8, pa1, 0); \
      ob = MFMA16(v1, BF(pb1), ob); SB(); v1 = *(const LDSP bf16x8*)((VC) + VS);        EXP4(sa, 12, pa1, 2); \
      sa = MFMA16(k0, qa0, f32x16{}); SB();                                             EXP4(sb, 0, pb0, 0); \
      oa = MFMA16(v0, BF(pa0), oa); SB();                                               EXP4(sb, 4, pb0, 2); \
      sa = MFMA8(k1, qa1, sa); SB();                                                    EXP4(sb, 8, pb1, 0); \
      oa = MFMA16(v1, BF(pa1), oa); SB();                                               EXP4(sb, 12, pb1, 2); \
    } while (0)
    for (int c = 0; c < NCH; ++c) {
      if (c > 0) {
        if (NBUF == 4 && c < NCH - 2) asm volatile("s_waitcnt vmcnt(3) lgkmcnt(0)\n\ts_barrier" ::: "memory");
        else asm volatile("s_waitcnt vmcnt(0) lgkmcnt(0)\n\ts_barrier" ::: "memory");
      }
      if (c + NBUF - 1 < NCH) DMA_CHUNK(c + NBUF - 1);
      const unsigned base = tb + (c % NBUF) * CH;
      const unsigned basen = tb + ((c + 1) % NBUF) * CH;
      const LDSP char* pk0 = (const LDSP char*)(base + ak0);
      const LDSP char* pk1 = (const LDSP char*)(base + ak1);
      const LDSP char* pv = (const LDSP char*)(base + av);
      const LDSP char* nk0 = (const LDSP char*)(basen + ak0);
      const LDSP char* nk1 = (const LDSP char*)(basen + ak1);
#pragma unroll
      for (int t = 0; t < TPC; ++t)
        STEP(t + 1 < TPC ? pk0 + (t + 1) * REC : nk0, t + 1 < TPC ? pk1 + (t + 1) * REC : nk1, pv + t * REC);
    }
    ob = MFMA16(v0, BF(pb0), ob); ob = MFMA16(v1, BF(pb1), ob);
    PROF_MARK2(2)
    if (KS == 2) {
      asm volatile("s_waitcnt vmcnt(0) lgkmcnt(0)\n\ts_barrier" ::: "memory");
      const unsigned red = lds0 + wq * 8192 + lane * 16;
      if (kh == 1) {
#pragma unroll
        for (int qd = 0; qd < 3; ++qd) { *(LDSP f32x4*)(red + qd * 1024) = f32x4{oa[4 * qd], oa[4 * qd + 1], oa[4 * qd + 2], oa[4 * qd + 3]};
                                         *(LDSP f32x4*)(red + 4096 + qd * 1024) = f32x4{ob[4 * qd], ob[4 * qd + 1], ob[4 * qd + 2], ob[4 * qd + 3]}; }
      }
      asm volatile("s_waitcnt lgkmcnt(0)\n\ts_barrier" ::: "memory");
      if (kh == 0) {
#pragma unroll
        for (int qd = 0; qd < 3; ++qd) { const f32x4 ta = *(const LDSP f32x4*)(red + qd * 1024), tb2 = *(const LDSP f32x4*)(red + 4096 + qd * 1024);
#pragma unroll
          for (int e = 0; e < 4; ++e) { oa[4 * qd + e] += ta[e]; ob[4 * qd + e] += tb2[e]; } }
      }
    }
    bool bad = false;
    if (kh == 0) {
#pragma unroll
      for (int qb = 0; qb < 2; ++qb) {
        const f32x16& o = qb ? ob : oa;
        auto rr = __builtin_amdgcn_permlane32_swap(__float_as_uint(o[8]), __float_as_uint(o[8]), false, false);
        const float l = __uint_as_float(rr[1]);
        bad |= !(l <= 1.099511627776e12f);
        const float inv = 1.0f / l;
        u16* op = (u16*)(ws + WS_O) + ((size_t)br * S + rb * 512 + wq * 64 + qb * 32 + r) * D + head * DH;
        *(u32x2*)(op + 4 * h) = u32x2{cvtpk(o[0] * inv, o[1] * inv), cvtpk(o[2] * inv, o[3] * inv)};
        *(u32x2*)(op + 8 + 4 * h) = u32x2{cvtpk(o[4] * inv, o[5] * inv), cvtpk(o[6] * inv, o[7] * inv)};
        if (h == 0) *(u32x2*)(op + 16) = u32x2{cvtpk(o[8] * inv, o[9] * inv), cvtpk(o[10] * inv, o[11] * inv)};
      }
    }
    if (attempt == 1) break;
    if (__any(bad)) *flag = 1;
    asm volatile("s_waitcnt lgkmcnt(0)\n\ts_barrier" ::: "memory");
    if (*flag == 0) break;
    asm volatile("s_waitcnt vmcnt(0) lgkmcnt(0)\n\ts_barrier" ::: "memory");
    DMA_CHUNK(0); DMA_CHUNK(1);
    if (NBUF == 4) DMA_CHUNK(2);
  }
  PROF_END(2, ws)
#undef DMA_CHUNK
}

extern "C" void kernel_launch(void* const* d_in, const int* in_sizes, int n_in, void* d_out, int out_size, void* d_ws, size_t ws_size, hipStream_t stream) {
  const u16* xa = (const u16*)d_in[0]; const u16* wqkv = (const u16*)d_in[1]; const u16* woa = (const u16*)d_in[2];
  const u16* xb = (const u16*)d_in[3]; const u16* wq = (const u16*)d_in[4]; const u16* wk = (const u16*)d_in[5];
  const u16* wv = (const u16*)d_in[6]; const u16* wob = (const u16*)d_in[7];
  char* ws = (char*)d_ws;
  (void)hipFuncSetAttribute((const void*)proj_kernel, hipFuncAttributeMaxDynamicSharedMemorySize, PROJ_LDS);
  (void)hipFuncSetAttribute((const void*)attn_kernel, hipFuncAttributeMaxDynamicSharedMemorySize, ATT_LDS);
  (void)hipFuncSetAttribute((const void*)oproj_kernel, hipFuncAttributeMaxDynamicSharedMemorySize, OPROJ_LDS);
  proj_kernel<<<256, 512, PROJ_LDS, stream>>>(xa, wqkv, xb, wq, wk, wv, ws);
  attn_kernel<<<256, 64 * AW, ATT_LDS, stream>>>(ws, ws);
  oproj_kernel<<<256, 512, OPROJ_LDS, stream>>>((const u16*)(ws + WS_O), woa, wob, (u16*)d_out);
}
```

```cpp
#define PROF 1
#include <stdio.h>
#include <hip/hip_runtime.h>
#include <hip/hip_bf16.h>
#include <stdint.h>

typedef unsigned short u16;
using bf16x8 = __attribute__((ext_vector_type(8))) short;
using bf16x4 = __attribute__((ext_vector_type(4))) short;
using f32x16 = __attribute__((ext_vector_type(16))) float;
using u32x4 = __attribute__((ext_vector_type(4))) unsigned;
using u32x2 = __attribute__((ext_vector_type(2))) unsigned;
using f32x4 = __attribute__((ext_vector_type(4))) float;
typedef float f32x2 __attribute__((ext_vector_type(2)));
typedef __bf16 bfv2 __attribute__((ext_vector_type(2)));
typedef short v4i16_t __attribute__((ext_vector_type(4)));
#define DI __device__ __forceinline__
#define LDSP __attribute__((address_space(3)))

constexpr int S = 2048, D = 640, H = 32, DH = 20;
constexpr int NT = S / 32;
constexpr int RB = 22;
constexpr int VOFF = 1296;
constexpr int VS = 2 * RB * 16;
constexpr int REC = VOFF + 4 * RB * 16;
constexpr int QREC = 1280;
constexpr size_t WS_KVA = 0;
constexpr size_t WS_KVB = WS_KVA + (size_t)H * NT * REC;
constexpr size_t WS_Q = WS_KVB + (size_t)NT * REC + 4096;
constexpr size_t WS_O = WS_Q + (size_t)2 * H * NT * QREC + 4096;
constexpr size_t WS_END = WS_O + (size_t)2 * S * D * 2;
constexpr float QSCALE = 0.22360679774997896f * 1.4426950408889634f;

DI unsigned cvtpk(float lo, float hi) { f32x2 v = {lo, hi}; bfv2 b = __builtin_convertvector(v, bfv2); return __builtin_bit_cast(unsigned, b); }
DI void glds16(const void* g, unsigned lds_base) {
  unsigned sv;
  asm volatile("s_mov_b32 %0, m0\n\ts_mov_b32 m0, %2\n\ts_nop 0\n\tglobal_load_lds_dwordx4 %1, off\n\ts_mov_b32 m0, %0" : "=&s"(sv) : "v"(g), "s"(lds_base) : "memory");
}
DI void glds16s(const void* sbase, unsigned voff, unsigned lds_base) {
  unsigned sv;
  asm volatile("s_mov_b32 %0, m0\n\ts_mov_b32 m0, %3\n\ts_nop 0\n\tglobal_load_lds_dwordx4 %1, %2\n\ts_mov_b32 m0, %0" : "=&s"(sv) : "v"(voff), "s"(sbase), "s"(lds_base) : "memory");
}
#define MFMA16(a, b, c) __builtin_amdgcn_mfma_f32_32x32x16_bf16(a, b, c, 0, 0, 0)
#define MFMA8(a, b, c) __builtin_amdgcn_mfma_f32_32x32x8bf16_1k(a, b, c, 0, 0, 0)
#define SB() __builtin_amdgcn_sched_barrier(0)
#define PIN(x) asm volatile("" : "+v"(x))
DI unsigned rfl(unsigned x) { return (unsigned)__builtin_amdgcn_readfirstlane((int)x); }
DI bf16x4 trread(unsigned a) { return __builtin_bit_cast(bf16x4, __builtin_amdgcn_ds_read_tr16_b64_v4i16((LDSP v4i16_t*)a)); }

constexpr int BK = 64, NKC = D / BK, NST = 4;
constexpr int WROW = 320;
constexpr int WBYTES = BK * WROW;
constexpr int EBYTES = 3072;
DI int colmap_head(int ct, int g) {
  const int o = g >> 2, hi = g & 1, u = (g >> 1) & 1;
  return ct < 4 ? 20 * (ct + 4 * o) + 8 * hi + 4 * u : 20 * (4 * o + 2 * hi + u) + 16;
}
DI int colmap_oct(int ct, int g) { return 32 * ct + 16 * (g >> 2) + 8 * (g & 1) + 4 * ((g >> 1) & 1); }
DI int colmap_id(int ct, int g) { return 32 * ct + 4 * g; }

#define PROF_BEGIN(k) unsigned long long pt0 = __builtin_amdgcn_s_memrealtime(), pc0 = __builtin_readcyclecounter(), pc1 = 0, pc2 = 0;
#define PROF_MARK1(k) pc1 = __builtin_readcyclecounter();
#define PROF_MARK2(k) pc2 = __builtin_readcyclecounter();
#define PROF_END(k, wsp) if ((threadIdx.x & 63) == 0) { unsigned long long* pb = (unsigned long long*)((char*)(wsp) + (64u << 20) + ((size_t)k << 20)) + (blockIdx.x * 16 + (threadIdx.x >> 6)) * 8; \
    pb[0] = pt0; pb[1] = __builtin_amdgcn_s_memrealtime(); pb[2] = pc1 - pc0; pb[3] = pc2 - pc1; pb[4] = __builtin_readcyclecounter() - pc2; }
#define WAITV(n) asm volatile("s_waitcnt vmcnt(" #n ")" ::: "memory")
#define BARRIER() asm volatile("s_waitcnt lgkmcnt(0)\n\ts_barrier" ::: "memory")

template <int NK, int NA, int NSTG, int STG, bool V04, bool V5, bool ODD, class Mark>
DI void gemm_consume(f32x16 (&acc)[6], const unsigned lds0, const unsigned xa, const unsigned (&wa)[4], const int ks0, Mark&& mark) {
  bf16x8 xf[2][2], wf[2][4];
#define RDX(set, sb, ks) do { xf[set][0] = *(const LDSP bf16x8*)((sb) + (xa ^ ((ks) << 5))); xf[set][1] = *(const LDSP bf16x8*)((sb) + 4096 + (xa ^ ((ks) << 5))); } while (0)
#define RDW(set, sb, ks, i) do { const unsigned rs_ = (i) == 3 ? 40 : WROW; const unsigned a_ = (sb) + wa[i] + (16 * (ks)) * rs_; \
      const bf16x4 lo_ = trread(a_), hi_ = trread(a_ + 4 * rs_); wf[set][i] = __builtin_shufflevector(lo_, hi_, 0, 1, 2, 3, 4, 5, 6, 7); } while (0)
#define MM1(i, X, W, V) acc[i] = (V) ? MFMA16(X, W, acc[i]) : MFMA16(W, X, acc[i])
#define STEPG(cur, sb, ks) do { \
    MM1(0, xf[cur][0], wf[cur][0], V04); SB(); RDX((cur) ^ 1, sb, ks); SB(); \
    MM1(1, xf[cur][1], wf[cur][0], V04); SB(); RDW((cur) ^ 1, sb, ks, 0); SB(); \
    MM1(2, xf[cur][0], wf[cur][1], V04); SB(); RDW((cur) ^ 1, sb, ks, 1); SB(); \
    MM1(3, xf[cur][1], wf[cur][1], V04); SB(); RDW((cur) ^ 1, sb, ks, 2); SB(); \
    MM1(4, xf[cur][ODD ? 1 : 0], wf[cur][2], V04); SB(); if (NA == 6) { RDW((cur) ^ 1, sb, ks, 3); SB(); MM1(5, xf[cur][ODD ? 1 : 0], wf[cur][3], V5); SB(); } } while (0)
  asm volatile("s_barrier" ::: "memory");
  mark();
  RDX(0, lds0, ks0); RDW(0, lds0, ks0, 0); RDW(0, lds0, ks0, 1); RDW(0, lds0, ks0, 2); if (NA == 6) RDW(0, lds0, ks0, 3);
  unsigned sb = lds0; int slot = 0;
#pragma unroll 2
  for (int c = 0; c < NKC; ++c) {
#pragma unroll
    for (int k = 0; k + 1 < NK; ++k) STEPG(k & 1, sb, ks0 + k + 1);
    asm volatile("s_waitcnt lgkmcnt(0)\n\ts_barrier" ::: "memory");
    slot = slot + 1 == NSTG ? 0 : slot + 1; sb = lds0 + slot * STG;
    STEPG(1, sb, ks0);
  }
#undef RDX
#undef RDW
#undef MM1
#undef STEPG
}
template <int NSTG, int STG, int NI, class Issue>
DI void gemm_load(const unsigned lds0, Issue&& issue) {
#pragma unroll
  for (int c = 0; c < NSTG; ++c) issue(c, lds0 + c * STG);
  asm volatile("s_waitcnt vmcnt(%0)\n\ts_barrier" :: "n"((NSTG - 1) * NI) : "memory");
  unsigned sb = lds0; int slot = 0;
  for (int c = 0; c < NKC; ++c) {
    const int rem = NKC - 2 - c;
    if (rem >= NSTG - 2) asm volatile("s_waitcnt vmcnt(%0)\n\ts_barrier" :: "n"((NSTG - 2) * NI) : "memory");
    else if (NSTG > 3 && rem == NSTG - 3) asm volatile("s_waitcnt vmcnt(%0)\n\ts_barrier" :: "n"((NSTG - 3) * NI) : "memory");
    else if (NSTG > 4 && rem == NSTG - 4) asm volatile("s_waitcnt vmcnt(%0)\n\ts_barrier" :: "n"(NSTG > 4 ? (NSTG - 4) * NI : 0) : "memory");
    else asm volatile("s_waitcnt vmcnt(0)\n\ts_barrier" ::: "memory");
    if (c + NSTG < NKC) issue(c + NSTG, sb);
    slot = slot + 1 == NSTG ? 0 : slot + 1; sb = lds0 + slot * STG;
  }
}

template <int KIND, int EXTRA>
DI void proj_tile(const u16* __restrict__ x, const u16* __restrict__ w, int ldw, const u16* __restrict__ we, int row0,
                  char* qk_base  , size_t head_stride, int rec, char* e_base, char* lds, char* wsp) {
  PROF_BEGIN(1)
  constexpr int XBYTES = 128 * 128;
  constexpr int STG = XBYTES + WBYTES + EBYTES;
  const int tid = threadIdx.x, lane = tid & 63, r = lane & 31, h = lane >> 5;
  const int wid = __builtin_amdgcn_readfirstlane(tid >> 6);
  const unsigned lds0 = (unsigned)(uintptr_t)lds;
  if (wid >= 4) {
    const int lw = wid - 4;
    unsigned xo[4], wo[5];
#pragma unroll
    for (int i = 0; i < 4; ++i) { const int row = 8 * (4 * lw + i) + (lane >> 3); xo[i] = row * (D * 2) + (((lane & 7) ^ ((row >> 1) & 7)) << 4); }
#pragma unroll
    for (int i = 0; i < 5; ++i) { const int p = 64 * (5 * lw + i) + lane, row = p / 20, c = p - 20 * row; wo[i] = row * ldw * 2 + c * 16; }
    const char* xs = (const char*)x + (size_t)row0 * D * 2;
    const int ep = lw < 2 ? lw : 2;
    const unsigned eo = ep * 1024 + (ep == 2 ? (lane & 31) : lane) * 16;
    auto issue = [&](int c, unsigned sb) {
      const char* xc = xs + c * (BK * 2); const char* wc = (const char*)w + (size_t)c * BK * ldw * 2;
#pragma unroll
      for (int i = 0; i < 4; ++i) glds16s(xc, xo[i], sb + (4 * lw + i) * 1024);
#pragma unroll
      for (int i = 0; i < 5; ++i) glds16s(wc, wo[i], sb + XBYTES + (5 * lw + i) * 1024);
      if (EXTRA != 0) glds16s((const char*)we + c * 2560, eo, sb + XBYTES + WBYTES + ep * 1024);
    };
    gemm_load<NST, STG, EXTRA != 0 ? 10 : 9>(lds0, issue);
    PROF_END(1, wsp)
    return;
  }
  const int tp = wid >> 1, odd = wid & 1;
  const unsigned xa = tp * 8192 + r * 128 + ((h ^ ((r >> 1) & 7)) << 4);
  const int g = 4 * ((lane >> 4) & 1) + (lane & 3), q = (lane & 15) >> 2;
  unsigned wa[4];
#pragma unroll
  for (int i = 0; i < 3; ++i) { const int ct = i < 2 ? 2 * odd + i : 4; wa[i] = XBYTES + (8 * h + q) * WROW + (KIND == 2 ? colmap_id(ct, g) : colmap_head(ct, g)) * 2; }
  { int col;
    if (EXTRA == 1) { const int w8 = 2 * (g >> 2) + (g & 1), u = (g >> 1) & 1; col = w8 < 2 ? 8 * w8 + 4 * u : (w8 == 2 && u == 0 ? 16 : 0); }
    else col = g < 5 ? 4 * g : 0;
    wa[3] = XBYTES + WBYTES + (8 * h + q) * 40 + col * 2; }
  constexpr int NA = EXTRA != 0 ? 6 : 5;
  f32x16 acc[6];
#pragma unroll
  for (int i = 0; i < 6; ++i) acc[i] = f32x16{};
  auto mark = [&]() { PROF_MARK1(1) };
  if (odd) gemm_consume<4, NA, NST, STG, KIND == 2, EXTRA == 2, true>(acc, lds0, xa, wa, 0, mark);
  else gemm_consume<4, NA, NST, STG, KIND == 2, EXTRA == 2, false>(acc, lds0, xa, wa, 0, mark);
  PROF_MARK2(1)
#pragma unroll
  for (int i = 0; i < NA; ++i) {
    const bool ex = i == 5;
    const int ct = i < 4 ? 2 * odd + (i >> 1) : 4;
    const int tile = (row0 >> 5) + (i < 4 ? 2 * tp + (i & 1) : wid);
    if (ex ? (EXTRA == 2) : (KIND == 2)) {
      const int c = ex ? r : 32 * ct + r;
      const int hl = ex ? 0 : c / 20, d = ex ? r : c - 20 * hl;
      if (!ex || r < 20) {
        char* rb = (ex ? e_base : qk_base + hl * head_stride) + (size_t)tile * REC + VOFF + (h * RB) * 16;
#pragma unroll
        for (int s = 0; s < 2; ++s) {
          u32x4 v = {cvtpk(acc[i][8 * s], acc[i][8 * s + 1]), cvtpk(acc[i][8 * s + 2], acc[i][8 * s + 3]), cvtpk(acc[i][8 * s + 4], acc[i][8 * s + 5]), cvtpk(acc[i][8 * s + 6], acc[i][8 * s + 7])};
          *(u32x4*)(rb + s * VS + d * 16) = v;
          if (d == 0) {
            *(u32x4*)(rb + s * VS + 20 * 16) = u32x4{0x3F803F80u, 0x3F803F80u, 0x3F803F80u, 0x3F803F80u};
            *(u32x4*)(rb + s * VS + 21 * 16) = u32x4{0u, 0u, 0u, 0u};
          }
        }
      }
    } else {
      const bool isq = !ex && KIND == 0;
      const float sc = isq ? QSCALE : 1.0f;
#pragma unroll
      for (int o = 0; o < 2; ++o) {
        u32x4 v = {cvtpk(acc[i][8 * o] * sc, acc[i][8 * o + 1] * sc), cvtpk(acc[i][8 * o + 2] * sc, acc[i][8 * o + 3] * sc),
                   cvtpk(acc[i][8 * o + 4] * sc, acc[i][8 * o + 5] * sc), cvtpk(acc[i][8 * o + 6] * sc, acc[i][8 * o + 7] * sc)};
        if (ex) {
          char* rb = e_base + (size_t)tile * REC;
          if (o == 0) *(u32x4*)(rb + h * 512 + r * 16) = v;
          else if (h == 0) { *(u32x2*)(rb + 1024 + r * 8) = u32x2{v[0], v[1]}; if (r == 0) *(u32x2*)(rb + 1280) = u32x2{0x3F803F80u, 0x00003F80u}; }
        } else if (i < 4) {
          *(u32x4*)(qk_base + (size_t)(ct + 4 * o) * head_stride + (size_t)tile * rec + h * 512 + r * 16) = v;
        } else {
          char* rb = qk_base + (size_t)(4 * o + 2 * h) * head_stride + (size_t)tile * rec;
          *(u32x2*)(rb + 1024 + r * 8) = u32x2{v[0], v[1]};
          *(u32x2*)(rb + head_stride + 1024 + r * 8) = u32x2{v[2], v[3]};
          if (KIND == 1 && r == 0) { *(u32x2*)(rb + 1280) = u32x2{0x3F803F80u, 0x00003F80u}; *(u32x2*)(rb + head_stride + 1280) = u32x2{0x3F803F80u, 0x00003F80u}; }
        }
      }
    }
  }
  PROF_END(1, wsp)
}
constexpr int PROJ_LDS = NST * (128 * 128 + WBYTES + EBYTES);

__global__ __launch_bounds__(512) void proj_kernel(const u16* __restrict__ xa, const u16* __restrict__ wqkv, const u16* __restrict__ xb, const u16* __restrict__ wq,
                                                   const u16* __restrict__ wk, const u16* __restrict__ wv, char* __restrict__ ws) {
  extern __shared__ __attribute__((aligned(16))) char lds[];
  const int xg = blockIdx.x & 7, j = blockIdx.x >> 3;
  const int rblk = (xg & 1) * 8 + (j & 7), cq = j >> 3;
  const int row0 = rblk * 128;
  if (xg < 6) {
    const int kind = xg >> 1;
    const u16* w = wqkv + kind * 640 + cq * 160;
    if (kind == 0) proj_tile<0, 0>(xa, w, 1920, nullptr, row0, ws + WS_Q + (size_t)(cq * 8) * NT * QREC, (size_t)NT * QREC, QREC, nullptr, lds, ws);
    else if (kind == 1) proj_tile<1, 0>(xa, w, 1920, nullptr, row0, ws + WS_KVA + (size_t)(cq * 8) * NT * REC, (size_t)NT * REC, REC, nullptr, lds, ws);
    else proj_tile<2, 0>(xa, w, 1920, nullptr, row0, ws + WS_KVA + (size_t)(cq * 8) * NT * REC, (size_t)NT * REC, REC, nullptr, lds, ws);
  } else {
    char* qb = ws + WS_Q + (size_t)(H + cq * 8) * NT * QREC;
    const u16* w = wq + cq * 160;
    if (cq == 0) proj_tile<0, 1>(xb, w, 640, wk, row0, qb, (size_t)NT * QREC, QREC, ws + WS_KVB, lds, ws);
    else if (cq == 1) proj_tile<0, 2>(xb, w, 640, wv, row0, qb, (size_t)NT * QREC, QREC, ws + WS_KVB, lds, ws);
    else proj_tile<0, 0>(xb, w, 640, nullptr, row0, qb, (size_t)NT * QREC, QREC, nullptr, lds, ws);
  }
}

constexpr int ONST = 5;
constexpr int OSTG = 64 * 128 + WBYTES;
constexpr int OPROJ_LDS = ONST * OSTG;
__global__ __launch_bounds__(512) void oproj_kernel(const u16* __restrict__ o, const u16* __restrict__ woa, const u16* __restrict__ wob, u16* __restrict__ out) {
  extern __shared__ __attribute__((aligned(16))) char lds[];
  PROF_BEGIN(3)
  constexpr int XBYTES = 64 * 128;
  const int tid = threadIdx.x, lane = tid & 63, r = lane & 31, h = lane >> 5;
  const int wid = __builtin_amdgcn_readfirstlane(tid >> 6);
  const int xg = blockIdx.x & 7, j = blockIdx.x >> 3;
  const int br = xg >> 2, rblk = (xg & 3) * 8 + (j & 7), cq = j >> 3;
  const int row0 = rblk * 64;
  const unsigned lds0 = (unsigned)(uintptr_t)lds;
  if (wid >= 4) {
    const int lw = wid - 4;
    const char* xs = (const char*)(o + ((size_t)br * S + row0) * D);
    const char* w = (const char*)((br ? wob : woa) + cq * 160);
    unsigned xo[2], wo[5];
#pragma unroll
    for (int i = 0; i < 2; ++i) { const int row = 8 * (2 * lw + i) + (lane >> 3); xo[i] = row * (D * 2) + (((lane & 7) ^ ((row >> 1) & 7)) << 4); }
#pragma unroll
    for (int i = 0; i < 5; ++i) { const int p = 64 * (5 * lw + i) + lane, row = p / 20, c = p - 20 * row; wo[i] = row * (D * 2) + c * 16; }
    auto issue = [&](int c, unsigned sb) {
      const char* xc = xs + c * (BK * 2); const char* wc = w + (size_t)c * BK * D * 2;
#pragma unroll
      for (int i = 0; i < 2; ++i) glds16s(xc, xo[i], sb + (2 * lw + i) * 1024);
#pragma unroll
      for (int i = 0; i < 5; ++i) glds16s(wc, wo[i], sb + XBYTES + (5 * lw + i) * 1024);
    };
    gemm_load<ONST, OSTG, 7>(lds0, issue);
    asm volatile("s_barrier\n\ts_barrier" ::: "memory");
    PROF_END(3, (char*)o - WS_O)
    return;
  }
  const int odd = wid & 1, kh = wid >> 1;
  const unsigned xa = r * 128 + ((h ^ ((r >> 1) & 7)) << 4);
  const int g = 4 * ((lane >> 4) & 1) + (lane & 3), q = (lane & 15) >> 2;
  unsigned wa[4];
#pragma unroll
  for (int i = 0; i < 3; ++i) wa[i] = XBYTES + (8 * h + q) * WROW + colmap_oct(i < 2 ? 2 * odd + i : 4, g) * 2;
  wa[3] = 0;
  f32x16 acc[6];
#pragma unroll
  for (int i = 0; i < 6; ++i) acc[i] = f32x16{};
  auto mark = [&]() { PROF_MARK1(3) };
  if (odd) gemm_consume<2, 5, ONST, OSTG, false, false, true>(acc, lds0, xa, wa, 2 * kh, mark);
  else gemm_consume<2, 5, ONST, OSTG, false, false, false>(acc, lds0, xa, wa, 2 * kh, mark);
  PROF_MARK2(3)
  BARRIER();
  const unsigned red = lds0 + odd * (5 * 4096) + lane * 16;
  if (kh == 1) {
#pragma unroll
    for (int i = 0; i < 5; ++i)
#pragma unroll
      for (int qd = 0; qd < 4; ++qd) *(LDSP f32x4*)(red + i * 4096 + qd * 1024) = f32x4{acc[i][4 * qd], acc[i][4 * qd + 1], acc[i][4 * qd + 2], acc[i][4 * qd + 3]};
  }
  BARRIER();
  if (kh == 0) {
#pragma unroll
    for (int i = 0; i < 5; ++i) {
      const int ct = i < 4 ? 2 * odd + (i >> 1) : 4, tt = i < 4 ? (i & 1) : odd;
      u16* op = out + (size_t)(row0 + 32 * tt + r) * 1280 + br * 640 + cq * 160 + 32 * ct + 8 * h;
#pragma unroll
      for (int qd = 0; qd < 4; ++qd) { const f32x4 t = *(const LDSP f32x4*)(red + i * 4096 + qd * 1024);
        acc[i][4 * qd] += t[0]; acc[i][4 * qd + 1] += t[1]; acc[i][4 * qd + 2] += t[2]; acc[i][4 * qd + 3] += t[3]; }
#pragma unroll
      for (int o8 = 0; o8 < 2; ++o8) {
        u32x4 v = {cvtpk(acc[i][8 * o8], acc[i][8 * o8 + 1]), cvtpk(acc[i][8 * o8 + 2], acc[i][8 * o8 + 3]), cvtpk(acc[i][8 * o8 + 4], acc[i][8 * o8 + 5]), cvtpk(acc[i][8 * o8 + 6], acc[i][8 * o8 + 7])};
        *(u32x4*)(op + 16 * o8) = v;
      }
    }
  }
  PROF_END(3, (char*)o - WS_O)
}

#define MX3(a, b, c) __builtin_fmaxf(__builtin_fmaxf((a), (b)), (c))
DI float rowmax16(const f32x16& p) {
  float a = MX3(p[0], p[1], p[2]), b = MX3(p[3], p[4], p[5]);
  a = MX3(a, p[6], p[7]); b = MX3(b, p[8], p[9]); a = MX3(a, p[10], p[11]); b = MX3(b, p[12], p[13]); a = MX3(a, p[14], p[15]);
  return __builtin_fmaxf(a, b);
}
DI float xhalf_max(float m) {
  auto rr = __builtin_amdgcn_permlane32_swap(__float_as_uint(m), __float_as_uint(m), false, false);
  return __builtin_fmaxf(__uint_as_float(rr[0]), __uint_as_float(rr[1]));
}
DI bf16x4 negm_frag(float m) {
  const float n = -m;
  const unsigned h1 = cvtpk(n, n) & 0xffffu; const float r1 = n - __uint_as_float(h1 << 16);
  const unsigned h2 = cvtpk(r1, r1) & 0xffffu; const float r2 = r1 - __uint_as_float(h2 << 16);
  const unsigned h3 = cvtpk(r2, r2) & 0xffffu;
  u32x2 p = {h1 | (h2 << 16), h3};
  return __builtin_bit_cast(bf16x4, p);
}

#define KS 1
constexpr int TPC = 8;
constexpr int CTILES = TPC * KS;
constexpr int CH = CTILES * REC;
constexpr int NCH = NT / CTILES;
constexpr int NBUF = KS == 1 ? 4 : 3;
constexpr int AW = 8 * KS;
constexpr int QL_OFF = NBUF * CH + 64;
constexpr int ATT_LDS = QL_OFF + 16 * QREC + (KS == 2 ? 0 : 0);

__global__ __launch_bounds__(64 * AW) void attn_kernel(const char* __restrict__ ws_ro, char* __restrict__ ws) {
  extern __shared__ __attribute__((aligned(16))) char lds[];
  PROF_BEGIN(2)
  const int tid = threadIdx.x, lane = tid & 63, r = lane & 31, h = lane >> 5;
  const int wid = __builtin_amdgcn_readfirstlane(tid >> 6);
  const int wq = wid & 7, kh = wid >> 3;
  const int xg = blockIdx.x & 7, j = blockIdx.x >> 3;
  const int br = j >> 4, head = 4 * xg + ((j >> 2) & 3), rb = j & 3;
  const char* kv = ws_ro + (br ? WS_KVB : WS_KVA + (size_t)head * NT * REC);
  const char* qp = ws_ro + WS_Q + ((size_t)(br * H + head) * NT + rb * 16) * QREC;
  const unsigned lds0 = (unsigned)(uintptr_t)lds;
  volatile LDSP int* flag = (volatile LDSP int*)(lds0 + NBUF * CH);
  const char* dsrc = kv + wid * REC;
  const unsigned ddst = lds0 + wid * REC;
#define DMA_CHUNK(c) do { const char* s_ = dsrc + (size_t)(c) * CH; const unsigned d_ = rfl(ddst + ((c) % NBUF) * CH); \
    glds16s(s_, lane * 16, d_); glds16s(s_ + 1024, lane * 16, d_ + 1024); if (lane < 41) glds16s(s_ + 2048, lane * 16, d_ + 2048); } while (0)

  const int rm = r <= 21 ? r : 21;
  const unsigned ak0 = h * 512 + r * 16, ak1 = h ? 1280 : 1024 + r * 8, av = VOFF + (h * RB + rm) * 16;

  if (KS == 1) { glds16s(qp + wid * 2560, lane * 16, rfl(lds0 + QL_OFF + wid * 2560)); glds16s(qp + wid * 2560 + 1024, lane * 16, rfl(lds0 + QL_OFF + wid * 2560 + 1024));
                 if (lane < 32) glds16s(qp + wid * 2560 + 2048, lane * 16, rfl(lds0 + QL_OFF + wid * 2560 + 2048)); }
  else { glds16s(qp + wid * 1280, lane * 16, rfl(lds0 + QL_OFF + wid * 1280)); if (lane < 16) glds16s(qp + wid * 1280 + 1024, lane * 16, rfl(lds0 + QL_OFF + wid * 1280 + 1024)); }
  if (tid == 0) *flag = 0;
  DMA_CHUNK(0); DMA_CHUNK(1);
  if (NBUF == 4) DMA_CHUNK(2);
  bf16x8 qa0, qb0; bf16x4 qa1, qb1, qa1d, qb1d;
  f32x16 oa, ob;
  for (int attempt = 0; attempt < 2; ++attempt) {
    if (NBUF == 4) asm volatile("s_waitcnt vmcnt(6) lgkmcnt(0)\n\ts_barrier" ::: "memory");
    else asm volatile("s_waitcnt vmcnt(3) lgkmcnt(0)\n\ts_barrier" ::: "memory");
    {
      const unsigned ql = lds0 + QL_OFF + wq * 2560;
      qa0 = *(const LDSP bf16x8*)(ql + h * 512 + r * 16); qa1d = *(const LDSP bf16x4*)(ql + 1024 + r * 8);
      qb0 = *(const LDSP bf16x8*)(ql + 1280 + h * 512 + r * 16); qb1d = *(const LDSP bf16x4*)(ql + 1280 + 1024 + r * 8);
      const bf16x4 z4 = {0, 0, 0, 0};
      const bf16x4 qaz = h ? z4 : qa1d, qbz = h ? z4 : qb1d;
      float ma, mb;
      if (attempt == 0) {
        const bf16x8 kf0 = *(const LDSP bf16x8*)(lds0 + ak0); const bf16x4 kf1 = *(const LDSP bf16x4*)(lds0 + ak1);
        f32x16 s = MFMA16(kf0, qa0, f32x16{}); s = MFMA8(kf1, qaz, s); ma = xhalf_max(rowmax16(s));
        s = MFMA16(kf0, qb0, f32x16{}); s = MFMA8(kf1, qbz, s); mb = xhalf_max(rowmax16(s));
      } else {
        ma = -INFINITY; mb = -INFINITY;
        for (int t = 0; t < NT; ++t) {
          const char* rec = kv + (size_t)t * REC;
          const bf16x8 kf0 = *(const bf16x8*)(rec + ak0); const bf16x4 kf1 = *(const bf16x4*)(rec + ak1);
          f32x16 s = MFMA16(kf0, qa0, f32x16{}); s = MFMA8(kf1, qaz, s); ma = __builtin_fmaxf(ma, rowmax16(s));
          s = MFMA16(kf0, qb0, f32x16{}); s = MFMA8(kf1, qbz, s); mb = __builtin_fmaxf(mb, rowmax16(s));
        }
        ma = xhalf_max(ma); mb = xhalf_max(mb);
      }
      qa1 = h ? negm_frag(ma) : qa1d; qb1 = h ? negm_frag(mb) : qb1d;
    }
    oa = f32x16{}; ob = f32x16{};
    PROF_MARK1(2)
    f32x16 sa, sb; bf16x8 k0, v0, v1; bf16x4 k1; u32x4 pa0, pa1, pb0, pb1;
    const unsigned tb = lds0 + kh * TPC * REC;
    {
      if (NBUF == 4) asm volatile("s_waitcnt vmcnt(3)\n\ts_barrier" ::: "memory");
      else asm volatile("s_waitcnt vmcnt(0)\n\ts_barrier" ::: "memory");
      k0 = *(const LDSP bf16x8*)(tb + ak0); k1 = *(const LDSP bf16x4*)(tb + ak1);
      sa = MFMA16(k0, qa0, f32x16{}); sa = MFMA8(k1, qa1, sa);
      v0 = *(const LDSP bf16x8*)(tb + av); v1 = v0;
      pb0 = u32x4{0, 0, 0, 0}; pb1 = pb0;
    }
#define EX(x) __builtin_amdgcn_exp2f(x)
#define EXP4(SC, i, P, k) do { const float e0 = EX(SC[i]), e1 = EX(SC[i + 1]), e2 = EX(SC[i + 2]), e3 = EX(SC[i + 3]); P[k] = cvtpk(e0, e1); P[k + 1] = cvtpk(e2, e3); PIN(P); SB(); } while (0)
#define BF(x) __builtin_bit_cast(bf16x8, x)
#define STEP(KN0, KN1, VC) do { \
      sb = MFMA16(k0, qb0, f32x16{}); SB(); k0 = *(const LDSP bf16x8*)(KN0);            EXP4(sa, 0, pa0, 0); \
      ob = MFMA16(v0, BF(pb0), ob); SB(); v0 = *(const LDSP bf16x8*)(VC);               EXP4(sa, 4, pa0, 2); \
      sb = MFMA8(k1, qb1, sb); SB(); k1 = *(const LDSP bf16x4*)(KN1);                   EXP4(sa, 8, pa1, 0); \
      ob = MFMA16(v1, BF(pb1), ob); SB(); v1 = *(const LDSP bf16x8*)((VC) + VS);        EXP4(sa, 12, pa1, 2); \
      sa = MFMA16(k0, qa0, f32x16{}); SB();                                             EXP4(sb, 0, pb0, 0); \
      oa = MFMA16(v0, BF(pa0), oa); SB();                                               EXP4(sb, 4, pb0, 2); \
      sa = MFMA8(k1, qa1, sa); SB();                                                    EXP4(sb, 8, pb1, 0); \
      oa = MFMA16(v1, BF(pa1), oa); SB();                                               EXP4(sb, 12, pb1, 2); \
    } while (0)
    for (int c = 0; c < NCH; ++c) {
      if (c > 0) {
        if (NBUF == 4 && c < NCH - 2) asm volatile("s_waitcnt vmcnt(3) lgkmcnt(0)\n\ts_barrier" ::: "memory");
        else asm volatile("s_waitcnt vmcnt(0) lgkmcnt(0)\n\ts_barrier" ::: "memory");
      }
      if (c + NBUF - 1 < NCH) DMA_CHUNK(c + NBUF - 1);
      const unsigned base = tb + (c % NBUF) * CH;
      const unsigned basen = tb + ((c + 1) % NBUF) * CH;
      const LDSP char* pk0 = (const LDSP char*)(base + ak0);
      const LDSP char* pk1 = (const LDSP char*)(base + ak1);
      const LDSP char* pv = (const LDSP char*)(base + av);
      const LDSP char* nk0 = (const LDSP char*)(basen + ak0);
      const LDSP char* nk1 = (const LDSP char*)(basen + ak1);
#pragma unroll
      for (int t = 0; t < TPC; ++t)
        STEP(t + 1 < TPC ? pk0 + (t + 1) * REC : nk0, t + 1 < TPC ? pk1 + (t + 1) * REC : nk1, pv + t * REC);
    }
    ob = MFMA16(v0, BF(pb0), ob); ob = MFMA16(v1, BF(pb1), ob);
    PROF_MARK2(2)
    if (KS == 2) {
      asm volatile("s_waitcnt vmcnt(0) lgkmcnt(0)\n\ts_barrier" ::: "memory");
      const unsigned red = lds0 + wq * 8192 + lane * 16;
      if (kh == 1) {
#pragma unroll
        for (int qd = 0; qd < 3; ++qd) { *(LDSP f32x4*)(red + qd * 1024) = f32x4{oa[4 * qd], oa[4 * qd + 1], oa[4 * qd + 2], oa[4 * qd + 3]};
                                         *(LDSP f32x4*)(red + 4096 + qd * 1024) = f32x4{ob[4 * qd], ob[4 * qd + 1], ob[4 * qd + 2], ob[4 * qd + 3]}; }
      }
      asm volatile("s_waitcnt lgkmcnt(0)\n\ts_barrier" ::: "memory");
      if (kh == 0) {
#pragma unroll
        for (int qd = 0; qd < 3; ++qd) { const f32x4 ta = *(const LDSP f32x4*)(red + qd * 1024), tb2 = *(const LDSP f32x4*)(red + 4096 + qd * 1024);
#pragma unroll
          for (int e = 0; e < 4; ++e) { oa[4 * qd + e] += ta[e]; ob[4 * qd + e] += tb2[e]; } }
      }
    }
    bool bad = false;
    if (kh == 0) {
#pragma unroll
      for (int qb = 0; qb < 2; ++qb) {
        const f32x16& o = qb ? ob : oa;
        auto rr = __builtin_amdgcn_permlane32_swap(__float_as_uint(o[8]), __float_as_uint(o[8]), false, false);
        const float l = __uint_as_float(rr[1]);
        bad |= !(l <= 1.099511627776e12f);
        const float inv = 1.0f / l;
        u16* op = (u16*)(ws + WS_O) + ((size_t)br * S + rb * 512 + wq * 64 + qb * 32 + r) * D + head * DH;
        *(u32x2*)(op + 4 * h) = u32x2{cvtpk(o[0] * inv, o[1] * inv), cvtpk(o[2] * inv, o[3] * inv)};
        *(u32x2*)(op + 8 + 4 * h) = u32x2{cvtpk(o[4] * inv, o[5] * inv), cvtpk(o[6] * inv, o[7] * inv)};
        if (h == 0) *(u32x2*)(op + 16) = u32x2{cvtpk(o[8] * inv, o[9] * inv), cvtpk(o[10] * inv, o[11] * inv)};
      }
    }
    if (attempt == 1) break;
    if (__any(bad)) *flag = 1;
    asm volatile("s_waitcnt lgkmcnt(0)\n\ts_barrier" ::: "memory");
    if (*flag == 0) break;
    asm volatile("s_waitcnt vmcnt(0) lgkmcnt(0)\n\ts_barrier" ::: "memory");
    DMA_CHUNK(0); DMA_CHUNK(1);
    if (NBUF == 4) DMA_CHUNK(2);
  }
  PROF_END(2, ws)
#undef DMA_CHUNK
}

__global__ void mark_kernel(char* ws, int idx) { if (threadIdx.x == 0) ((unsigned long long*)(ws + (70u << 20)))[idx] = __builtin_amdgcn_s_memrealtime(); }
__global__ void report_kernel(char* ws) {
  if (threadIdx.x != 0) return;
  const unsigned long long tnow = __builtin_amdgcn_s_memrealtime();
  const unsigned long long tA = ((unsigned long long*)(ws + (70u << 20)))[0];
  const int nw[4] = {0, 4, 8, 4};
  for (int k = 1; k <= 3; ++k) {
    const unsigned long long* pb = (const unsigned long long*)(ws + (64u << 20) + ((size_t)k << 20));
    unsigned long long s0 = ~0ull, s1 = 0, e0 = ~0ull, e1 = 0, pro = 0, mn = 0, ep = 0, cyc = 0, rt = 0;
    for (int b = 0; b < 256; ++b) for (int w = 0; w < nw[k]; ++w) { const unsigned long long* p = pb + (b * 16 + w) * 8;
      s0 = p[0] < s0 ? p[0] : s0; s1 = p[0] > s1 ? p[0] : s1; e0 = p[1] < e0 ? p[1] : e0; e1 = p[1] > e1 ? p[1] : e1; pro += p[2]; mn += p[3]; ep += p[4]; cyc += p[2] + p[3] + p[4]; rt += p[1] - p[0]; }
    const int n = 256 * nw[k];
    printf("K%d start %d..%d end %d..%d (x10ns after marker) avg pro %d main %d epi %d clk %d MHz\n", k, (int)(s0 - tA), (int)(s1 - tA), (int)(e0 - tA), (int)(e1 - tA), (int)(pro / n), (int)(mn / n), (int)(ep / n), (int)(cyc * 100 / rt));
  }
  printf("report start %d\n", (int)(tnow - tA));
}
extern "C" void kernel_launch(void* const* d_in, const int* in_sizes, int n_in, void* d_out, int out_size, void* d_ws, size_t ws_size, hipStream_t stream) {
  const u16* xa = (const u16*)d_in[0]; const u16* wqkv = (const u16*)d_in[1]; const u16* woa = (const u16*)d_in[2];
  const u16* xb = (const u16*)d_in[3]; const u16* wq = (const u16*)d_in[4]; const u16* wk = (const u16*)d_in[5];
  const u16* wv = (const u16*)d_in[6]; const u16* wob = (const u16*)d_in[7];
  char* ws = (char*)d_ws;
  (void)hipFuncSetAttribute((const void*)proj_kernel, hipFuncAttributeMaxDynamicSharedMemorySize, PROJ_LDS);
  (void)hipFuncSetAttribute((const void*)attn_kernel, hipFuncAttributeMaxDynamicSharedMemorySize, ATT_LDS);
  (void)hipFuncSetAttribute((const void*)oproj_kernel, hipFuncAttributeMaxDynamicSharedMemorySize, OPROJ_LDS);
  mark_kernel<<<1, 64, 0, stream>>>(ws, 0);
  proj_kernel<<<256, 512, PROJ_LDS, stream>>>(xa, wqkv, xb, wq, wk, wv, ws);
  attn_kernel<<<256, 64 * AW, ATT_LDS, stream>>>(ws, ws);
  oproj_kernel<<<256, 512, OPROJ_LDS, stream>>>((const u16*)(ws + WS_O), woa, wob, (u16*)d_out);
  report_kernel<<<1, 64, 0, stream>>>(ws);
}
```

```cpp
#include <hip/hip_runtime.h>
#include <hip/hip_bf16.h>
#include <stdint.h>

typedef unsigned short u16;
using bf16x8 = __attribute__((ext_vector_type(8))) short;
using bf16x4 = __attribute__((ext_vector_type(4))) short;
using f32x16 = __attribute__((ext_vector_type(16))) float;
using u32x4 = __attribute__((ext_vector_type(4))) unsigned;
using u32x2 = __attribute__((ext_vector_type(2))) unsigned;
using f32x4 = __attribute__((ext_vector_type(4))) float;
typedef float f32x2 __attribute__((ext_vector_type(2)));
typedef __bf16 bfv2 __attribute__((ext_vector_type(2)));
typedef short v4i16_t __attribute__((ext_vector_type(4)));
#define DI __device__ __forceinline__
#define LDSP __attribute__((address_space(3)))

constexpr int S = 2048, D = 640, H = 32, DH = 20;
constexpr int NT = S / 32;
constexpr int RB = 22;
constexpr int VOFF = 1296;
constexpr int VS = 2 * RB * 16;
constexpr int REC = VOFF + 4 * RB * 16;
constexpr int QREC = 1280;
constexpr size_t WS_KVA = 0;
constexpr size_t WS_KVB = WS_KVA + (size_t)H * NT * REC;
constexpr size_t WS_Q = WS_KVB + (size_t)NT * REC + 4096;
constexpr size_t WS_O = WS_Q + (size_t)2 * H * NT * QREC + 4096;
constexpr size_t WS_END = WS_O + (size_t)2 * S * D * 2;
constexpr float QSCALE = 0.22360679774997896f * 1.4426950408889634f;

DI unsigned cvtpk(float lo, float hi) { f32x2 v = {lo, hi}; bfv2 b = __builtin_convertvector(v, bfv2); return __builtin_bit_cast(unsigned, b); }
DI void glds16(const void* g, unsigned lds_base) {
  unsigned sv;
  asm volatile("s_mov_b32 %0, m0\n\ts_mov_b32 m0, %2\n\ts_nop 0\n\tglobal_load_lds_dwordx4 %1, off\n\ts_mov_b32 m0, %0" : "=&s"(sv) : "v"(g), "s"(lds_base) : "memory");
}
DI void glds16s(const void* sbase, unsigned voff, unsigned lds_base) {
  unsigned sv;
  asm volatile("s_mov_b32 %0, m0\n\ts_mov_b32 m0, %3\n\ts_nop 0\n\tglobal_load_lds_dwordx4 %1, %2\n\ts_mov_b32 m0, %0" : "=&s"(sv) : "v"(voff), "s"(sbase), "s"(lds_base) : "memory");
}
DI void l2touch(const void* g, unsigned lds_dummy) {
  unsigned sv;
  asm volatile("s_mov_b32 %0, m0\n\ts_mov_b32 m0, %2\n\ts_nop 0\n\tglobal_load_lds_dword %1, off\n\ts_mov_b32 m0, %0" : "=&s"(sv) : "v"(g), "s"(lds_dummy) : "memory");
}
#define MFMA16(a, b, c) __builtin_amdgcn_mfma_f32_32x32x16_bf16(a, b, c, 0, 0, 0)
#define MFMA8(a, b, c) __builtin_amdgcn_mfma_f32_32x32x8bf16_1k(a, b, c, 0, 0, 0)
#define SB() __builtin_amdgcn_sched_barrier(0)
#define PIN(x) asm volatile("" : "+v"(x))
DI unsigned rfl(unsigned x) { return (unsigned)__builtin_amdgcn_readfirstlane((int)x); }
DI bf16x4 trread(unsigned a) { return __builtin_bit_cast(bf16x4, __builtin_amdgcn_ds_read_tr16_b64_v4i16((LDSP v4i16_t*)a)); }

constexpr int BK = 64, NKC = D / BK, NST = 4;
constexpr int WROW = 320;
constexpr int WBYTES = BK * WROW;
constexpr int EBYTES = 3072;
DI int colmap_head(int ct, int g) {
  const int o = g >> 2, hi = g & 1, u = (g >> 1) & 1;
  return ct < 4 ? 20 * (ct + 4 * o) + 8 * hi + 4 * u : 20 * (4 * o + 2 * hi + u) + 16;
}
DI int colmap_oct(int ct, int g) { return 32 * ct + 16 * (g >> 2) + 8 * (g & 1) + 4 * ((g >> 1) & 1); }
DI int colmap_id(int ct, int g) { return 32 * ct + 4 * g; }

#define PROF_BEGIN(k)
#define PROF_MARK1(k)
#define PROF_MARK2(k)
#define PROF_END(k, wsp)
#define WAITV(n) asm volatile("s_waitcnt vmcnt(" #n ")" ::: "memory")
#define BARRIER() asm volatile("s_waitcnt lgkmcnt(0)\n\ts_barrier" ::: "memory")

template <int NK, int NA, int NSTG, int STG, bool V04, bool V5, bool ODD, class Mark>
DI void gemm_consume(f32x16 (&acc)[6], const unsigned lds0, const unsigned xa, const unsigned (&wa)[4], const int ks0, Mark&& mark) {
  bf16x8 xf[2][2], wf[2][4];
#define RDX(set, sb, ks) do { xf[set][0] = *(const LDSP bf16x8*)((sb) + (xa ^ ((ks) << 5))); xf[set][1] = *(const LDSP bf16x8*)((sb) + 4096 + (xa ^ ((ks) << 5))); } while (0)
#define RDW(set, sb, ks, i) do { const unsigned rs_ = (i) == 3 ? 40 : WROW; const unsigned a_ = (sb) + wa[i] + (16 * (ks)) * rs_; \
      const bf16x4 lo_ = trread(a_), hi_ = trread(a_ + 4 * rs_); wf[set][i] = __builtin_shufflevector(lo_, hi_, 0, 1, 2, 3, 4, 5, 6, 7); } while (0)
#define MM1(i, X, W, V) acc[i] = (V) ? MFMA16(X, W, acc[i]) : MFMA16(W, X, acc[i])
#define STEPG(cur, sb, ks) do { \
    MM1(0, xf[cur][0], wf[cur][0], V04); SB(); RDX((cur) ^ 1, sb, ks); SB(); \
    MM1(1, xf[cur][1], wf[cur][0], V04); SB(); RDW((cur) ^ 1, sb, ks, 0); SB(); \
    MM1(2, xf[cur][0], wf[cur][1], V04); SB(); RDW((cur) ^ 1, sb, ks, 1); SB(); \
    MM1(3, xf[cur][1], wf[cur][1], V04); SB(); RDW((cur) ^ 1, sb, ks, 2); SB(); \
    MM1(4, xf[cur][ODD ? 1 : 0], wf[cur][2], V04); SB(); if (NA == 6) { RDW((cur) ^ 1, sb, ks, 3); SB(); MM1(5, xf[cur][ODD ? 1 : 0], wf[cur][3], V5); SB(); } } while (0)
  asm volatile("s_barrier" ::: "memory");
  mark();
  RDX(0, lds0, ks0); RDW(0, lds0, ks0, 0); RDW(0, lds0, ks0, 1); RDW(0, lds0, ks0, 2); if (NA == 6) RDW(0, lds0, ks0, 3);
  unsigned sb = lds0; int slot = 0;
#pragma unroll 2
  for (int c = 0; c < NKC; ++c) {
#pragma unroll
    for (int k = 0; k + 1 < NK; ++k) STEPG(k & 1, sb, ks0 + k + 1);
    asm volatile("s_waitcnt lgkmcnt(0)\n\ts_barrier" ::: "memory");
    slot = slot + 1 == NSTG ? 0 : slot + 1; sb = lds0 + slot * STG;
    STEPG(1, sb, ks0);
  }
#undef RDX
#undef RDW
#undef MM1
#undef STEPG
}
template <int NSTG, int STG, int NI, class Issue>
DI void gemm_load(const unsigned lds0, Issue&& issue) {
  issue(0, lds0); issue(1, lds0 + STG);
  asm volatile("s_waitcnt vmcnt(%0)" :: "n"(NI) : "memory");
  asm volatile("s_barrier" ::: "memory");
#pragma unroll
  for (int c = 2; c < NSTG; ++c) issue(c, lds0 + c * STG);
  unsigned sb = lds0; int slot = 0;
#pragma unroll
  for (int c = 0; c < NKC; ++c) {
    const int rem = NKC - 2 - c;
    if (rem >= NSTG - 2) asm volatile("s_waitcnt vmcnt(%0)\n\ts_barrier" :: "n"((NSTG - 2) * NI) : "memory");
    else if (NSTG > 3 && rem == NSTG - 3) asm volatile("s_waitcnt vmcnt(%0)\n\ts_barrier" :: "n"((NSTG - 3) * NI) : "memory");
    else if (NSTG > 4 && rem == NSTG - 4) asm volatile("s_waitcnt vmcnt(%0)\n\ts_barrier" :: "n"(NSTG > 4 ? (NSTG - 4) * NI : 0) : "memory");
    else asm volatile("s_waitcnt vmcnt(0)\n\ts_barrier" ::: "memory");
    if (c + NSTG < NKC) issue(c + NSTG, sb);
    slot = slot + 1 == NSTG ? 0 : slot + 1; sb = lds0 + slot * STG;
  }
}

template <int KIND, int EXTRA>
DI void proj_tile(const u16* __restrict__ x, const u16* __restrict__ w, int ldw, const u16* __restrict__ we, int row0,
                  char* qk_base  , size_t head_stride, int rec, char* e_base, char* lds, char* wsp,
                  const int koff) {
  PROF_BEGIN(1)
  constexpr int XBYTES = 128 * 128;
  constexpr int STG = XBYTES + WBYTES + EBYTES;
  const int tid = threadIdx.x, lane = tid & 63, r = lane & 31, h = lane >> 5;
  const int wid = __builtin_amdgcn_readfirstlane(tid >> 6);
  const unsigned lds0 = (unsigned)(uintptr_t)lds;
  if (wid >= 4) {
    const int lw = wid - 4;
    unsigned xo[4], wo[5];
#pragma unroll
    for (int i = 0; i < 4; ++i) { const int row = 8 * (4 * lw + i) + (lane >> 3); xo[i] = row * (D * 2) + (((lane & 7) ^ ((row >> 1) & 7)) << 4); }
#pragma unroll
    for (int i = 0; i < 5; ++i) { const int p = 64 * (5 * lw + i) + lane, row = p / 20, c = p - 20 * row; wo[i] = row * ldw * 2 + c * 16; }
    const char* xs = (const char*)x + (size_t)row0 * D * 2;
    const int ep = lw < 2 ? lw : 2;
    const unsigned eo = ep * 1024 + (ep == 2 ? (lane & 31) : lane) * 16;
    auto issue = [&](int ci, unsigned sb) {
      int c = ci + koff; if (c >= NKC) c -= NKC;
      const char* xc = xs + c * (BK * 2); const char* wc = (const char*)w + (size_t)c * BK * ldw * 2;
#pragma unroll
      for (int i = 0; i < 4; ++i) glds16s(xc, xo[i], sb + (4 * lw + i) * 1024);
#pragma unroll
      for (int i = 0; i < 5; ++i) glds16s(wc, wo[i], sb + XBYTES + (5 * lw + i) * 1024);
      if (EXTRA != 0) glds16s((const char*)we + c * 2560, eo, sb + XBYTES + WBYTES + ep * 1024);
    };
    gemm_load<NST, STG, EXTRA != 0 ? 10 : 9>(lds0, issue);
    PROF_END(1, wsp)
    return;
  }
  const int tp = wid >> 1, odd = wid & 1;
  const unsigned xa = tp * 8192 + r * 128 + ((h ^ ((r >> 1) & 7)) << 4);
  const int g = 4 * ((lane >> 4) & 1) + (lane & 3), q = (lane & 15) >> 2;
  unsigned wa[4];
#pragma unroll
  for (int i = 0; i < 3; ++i) { const int ct = i < 2 ? 2 * odd + i : 4; wa[i] = XBYTES + (8 * h + q) * WROW + (KIND == 2 ? colmap_id(ct, g) : colmap_head(ct, g)) * 2; }
  { int col;
    if (EXTRA == 1) { const int w8 = 2 * (g >> 2) + (g & 1), u = (g >> 1) & 1; col = w8 < 2 ? 8 * w8 + 4 * u : (w8 == 2 && u == 0 ? 16 : 0); }
    else col = g < 5 ? 4 * g : 0;
    wa[3] = XBYTES + WBYTES + (8 * h + q) * 40 + col * 2; }
  constexpr int NA = EXTRA != 0 ? 6 : 5;
  f32x16 acc[6];
#pragma unroll
  for (int i = 0; i < 6; ++i) acc[i] = f32x16{};
  auto mark = [&]() { PROF_MARK1(1) };
  if (odd) gemm_consume<4, NA, NST, STG, KIND == 2, EXTRA == 2, true>(acc, lds0, xa, wa, 0, mark);
  else gemm_consume<4, NA, NST, STG, KIND == 2, EXTRA == 2, false>(acc, lds0, xa, wa, 0, mark);
  PROF_MARK2(1)
#pragma unroll
  for (int i = 0; i < NA; ++i) {
    const bool ex = i == 5;
    const int ct = i < 4 ? 2 * odd + (i >> 1) : 4;
    const int tile = (row0 >> 5) + (i < 4 ? 2 * tp + (i & 1) : wid);
    if (ex ? (EXTRA == 2) : (KIND == 2)) {
      const int c = ex ? r : 32 * ct + r;
      const int hl = ex ? 0 : c / 20, d = ex ? r : c - 20 * hl;
      if (!ex || r < 20) {
        char* rb = (ex ? e_base : qk_base + hl * head_stride) + (size_t)tile * REC + VOFF + (h * RB) * 16;
#pragma unroll
        for (int s = 0; s < 2; ++s) {
          u32x4 v = {cvtpk(acc[i][8 * s], acc[i][8 * s + 1]), cvtpk(acc[i][8 * s + 2], acc[i][8 * s + 3]), cvtpk(acc[i][8 * s + 4], acc[i][8 * s + 5]), cvtpk(acc[i][8 * s + 6], acc[i][8 * s + 7])};
          *(u32x4*)(rb + s * VS + d * 16) = v;
          if (d == 0) {
            *(u32x4*)(rb + s * VS + 20 * 16) = u32x4{0x3F803F80u, 0x3F803F80u, 0x3F803F80u, 0x3F803F80u};
            *(u32x4*)(rb + s * VS + 21 * 16) = u32x4{0u, 0u, 0u, 0u};
          }
        }
      }
    } else {
      const bool isq = !ex && KIND == 0;
      const float sc = isq ? QSCALE : 1.0f;
#pragma unroll
      for (int o = 0; o < 2; ++o) {
        u32x4 v = {cvtpk(acc[i][8 * o] * sc, acc[i][8 * o + 1] * sc), cvtpk(acc[i][8 * o + 2] * sc, acc[i][8 * o + 3] * sc),
                   cvtpk(acc[i][8 * o + 4] * sc, acc[i][8 * o + 5] * sc), cvtpk(acc[i][8 * o + 6] * sc, acc[i][8 * o + 7] * sc)};
        if (ex) {
          char* rb = e_base + (size_t)tile * REC;
          if (o == 0) *(u32x4*)(rb + h * 512 + r * 16) = v;
          else if (h == 0) { *(u32x2*)(rb + 1024 + r * 8) = u32x2{v[0], v[1]}; if (r == 0) *(u32x2*)(rb + 1280) = u32x2{0x3F803F80u, 0x00003F80u}; }
        } else if (i < 4) {
          *(u32x4*)(qk_base + (size_t)(ct + 4 * o) * head_stride + (size_t)tile * rec + h * 512 + r * 16) = v;
        } else {
          char* rb = qk_base + (size_t)(4 * o + 2 * h) * head_stride + (size_t)tile * rec;
          *(u32x2*)(rb + 1024 + r * 8) = u32x2{v[0], v[1]};
          *(u32x2*)(rb + head_stride + 1024 + r * 8) = u32x2{v[2], v[3]};
          if (KIND == 1 && r == 0) { *(u32x2*)(rb + 1280) = u32x2{0x3F803F80u, 0x00003F80u}; *(u32x2*)(rb + head_stride + 1280) = u32x2{0x3F803F80u, 0x00003F80u}; }
        }
      }
    }
  }
  PROF_END(1, wsp)
}
constexpr int PROJ_LDS = NST * (128 * 128 + WBYTES + EBYTES) + 256;

__global__ __launch_bounds__(512) void proj_kernel(const u16* __restrict__ xa, const u16* __restrict__ wqkv, const u16* __restrict__ xb, const u16* __restrict__ wq,
                                                   const u16* __restrict__ wk, const u16* __restrict__ wv, char* __restrict__ ws) {
  extern __shared__ __attribute__((aligned(16))) char lds[];
  const int xg = blockIdx.x & 7, j = blockIdx.x >> 3;
  const int rblk = (xg & 1) * 8 + (j & 7), cq = j >> 3;
  const int row0 = rblk * 128;
  const int koff = ((j & 7) + 3 * cq) % NKC;
  if (xg < 6) {
    const int kind = xg >> 1;
    const u16* w = wqkv + kind * 640 + cq * 160;
    if (kind == 0) proj_tile<0, 0>(xa, w, 1920, nullptr, row0, ws + WS_Q + (size_t)(cq * 8) * NT * QREC, (size_t)NT * QREC, QREC, nullptr, lds, ws, koff);
    else if (kind == 1) proj_tile<1, 0>(xa, w, 1920, nullptr, row0, ws + WS_KVA + (size_t)(cq * 8) * NT * REC, (size_t)NT * REC, REC, nullptr, lds, ws, koff);
    else proj_tile<2, 0>(xa, w, 1920, nullptr, row0, ws + WS_KVA + (size_t)(cq * 8) * NT * REC, (size_t)NT * REC, REC, nullptr, lds, ws, koff);
  } else {
    char* qb = ws + WS_Q + (size_t)(H + cq * 8) * NT * QREC;
    const u16* w = wq + cq * 160;
    if (cq == 0) proj_tile<0, 1>(xb, w, 640, wk, row0, qb, (size_t)NT * QREC, QREC, ws + WS_KVB, lds, ws, koff);
    else if (cq == 1) proj_tile<0, 2>(xb, w, 640, wv, row0, qb, (size_t)NT * QREC, QREC, ws + WS_KVB, lds, ws, koff);
    else proj_tile<0, 0>(xb, w, 640, nullptr, row0, qb, (size_t)NT * QREC, QREC, nullptr, lds, ws, koff);
  }
}

constexpr int ONST = 5;
constexpr int OSTG = 64 * 128 + WBYTES;
constexpr int OPROJ_LDS = ONST * OSTG + 256;
__global__ __launch_bounds__(512) void oproj_kernel(const u16* __restrict__ o, const u16* __restrict__ woa, const u16* __restrict__ wob, u16* __restrict__ out) {
  extern __shared__ __attribute__((aligned(16))) char lds[];
  PROF_BEGIN(3)
  constexpr int XBYTES = 64 * 128;
  const int tid = threadIdx.x, lane = tid & 63, r = lane & 31, h = lane >> 5;
  const int wid = __builtin_amdgcn_readfirstlane(tid >> 6);
  const int xg = blockIdx.x & 7, j = blockIdx.x >> 3;
  const int br = xg >> 2, rblk = (xg & 3) * 8 + (j & 7), cq = j >> 3;
  const int row0 = rblk * 64;
  const unsigned lds0 = (unsigned)(uintptr_t)lds;
  if (wid >= 4) {
    const int lw = wid - 4;
    const char* xs = (const char*)(o + ((size_t)br * S + row0) * D);
    const char* w = (const char*)((br ? wob : woa) + cq * 160);
    unsigned xo[2], wo[5];
#pragma unroll
    for (int i = 0; i < 2; ++i) { const int row = 8 * (2 * lw + i) + (lane >> 3); xo[i] = row * (D * 2) + (((lane & 7) ^ ((row >> 1) & 7)) << 4); }
#pragma unroll
    for (int i = 0; i < 5; ++i) { const int p = 64 * (5 * lw + i) + lane, row = p / 20, c = p - 20 * row; wo[i] = row * (D * 2) + c * 16; }
    const int koff = ((j & 7) + 3 * cq) % NKC;
    auto issue = [&](int ci, unsigned sb) {
      int c = ci + koff; if (c >= NKC) c -= NKC;
      const char* xc = xs + c * (BK * 2); const char* wc = w + (size_t)c * BK * D * 2;
#pragma unroll
      for (int i = 0; i < 2; ++i) glds16s(xc, xo[i], sb + (2 * lw + i) * 1024);
#pragma unroll
      for (int i = 0; i < 5; ++i) glds16s(wc, wo[i], sb + XBYTES + (5 * lw + i) * 1024);
    };
    gemm_load<ONST, OSTG, 7>(lds0, issue);
    asm volatile("s_barrier\n\ts_barrier" ::: "memory");
    PROF_END(3, (char*)o - WS_O)
    return;
  }
  const int odd = wid & 1, kh = wid >> 1;
  const unsigned xa = r * 128 + ((h ^ ((r >> 1) & 7)) << 4);
  const int g = 4 * ((lane >> 4) & 1) + (lane & 3), q = (lane & 15) >> 2;
  unsigned wa[4];
#pragma unroll
  for (int i = 0; i < 3; ++i) wa[i] = XBYTES + (8 * h + q) * WROW + colmap_oct(i < 2 ? 2 * odd + i : 4, g) * 2;
  wa[3] = 0;
  f32x16 acc[6];
#pragma unroll
  for (int i = 0; i < 6; ++i) acc[i] = f32x16{};
  auto mark = [&]() { PROF_MARK1(3) };
  if (odd) gemm_consume<2, 5, ONST, OSTG, false, false, true>(acc, lds0, xa, wa, 2 * kh, mark);
  else gemm_consume<2, 5, ONST, OSTG, false, false, false>(acc, lds0, xa, wa, 2 * kh, mark);
  PROF_MARK2(3)
  BARRIER();
  const unsigned red = lds0 + odd * (5 * 4096) + lane * 16;
  if (kh == 1) {
#pragma unroll
    for (int i = 0; i < 5; ++i)
#pragma unroll
      for (int qd = 0; qd < 4; ++qd) *(LDSP f32x4*)(red + i * 4096 + qd * 1024) = f32x4{acc[i][4 * qd], acc[i][4 * qd + 1], acc[i][4 * qd + 2], acc[i][4 * qd + 3]};
  }
  BARRIER();
  if (kh == 0) {
#pragma unroll
    for (int i = 0; i < 5; ++i) {
      const int ct = i < 4 ? 2 * odd + (i >> 1) : 4, tt = i < 4 ? (i & 1) : odd;
      u16* op = out + (size_t)(row0 + 32 * tt + r) * 1280 + br * 640 + cq * 160 + 32 * ct + 8 * h;
#pragma unroll
      for (int qd = 0; qd < 4; ++qd) { const f32x4 t = *(const LDSP f32x4*)(red + i * 4096 + qd * 1024);
        acc[i][4 * qd] += t[0]; acc[i][4 * qd + 1] += t[1]; acc[i][4 * qd + 2] += t[2]; acc[i][4 * qd + 3] += t[3]; }
#pragma unroll
      for (int o8 = 0; o8 < 2; ++o8) {
        u32x4 v = {cvtpk(acc[i][8 * o8], acc[i][8 * o8 + 1]), cvtpk(acc[i][8 * o8 + 2], acc[i][8 * o8 + 3]), cvtpk(acc[i][8 * o8 + 4], acc[i][8 * o8 + 5]), cvtpk(acc[i][8 * o8 + 6], acc[i][8 * o8 + 7])};
        *(u32x4*)(op + 16 * o8) = v;
      }
    }
  }
  PROF_END(3, (char*)o - WS_O)
}

#define MX3(a, b, c) __builtin_fmaxf(__builtin_fmaxf((a), (b)), (c))
DI float rowmax16(const f32x16& p) {
  float a = MX3(p[0], p[1], p[2]), b = MX3(p[3], p[4], p[5]);
  a = MX3(a, p[6], p[7]); b = MX3(b, p[8], p[9]); a = MX3(a, p[10], p[11]); b = MX3(b, p[12], p[13]); a = MX3(a, p[14], p[15]);
  return __builtin_fmaxf(a, b);
}
DI float xhalf_max(float m) {
  auto rr = __builtin_amdgcn_permlane32_swap(__float_as_uint(m), __float_as_uint(m), false, false);
  return __builtin_fmaxf(__uint_as_float(rr[0]), __uint_as_float(rr[1]));
}
DI bf16x4 negm_frag(float m) {
  const float n = -m;
  const unsigned h1 = cvtpk(n, n) & 0xffffu; const float r1 = n - __uint_as_float(h1 << 16);
  const unsigned h2 = cvtpk(r1, r1) & 0xffffu; const float r2 = r1 - __uint_as_float(h2 << 16);
  const unsigned h3 = cvtpk(r2, r2) & 0xffffu;
  u32x2 p = {h1 | (h2 << 16), h3};
  return __builtin_bit_cast(bf16x4, p);
}

#define KS 1
constexpr int TPC = 8;
constexpr int CTILES = TPC * KS;
constexpr int CH = CTILES * REC;
constexpr int NCH = NT / CTILES;
constexpr int NBUF = KS == 1 ? 4 : 3;
constexpr int AW = 8 * KS;
constexpr int QL_OFF = NBUF * CH + 64;
constexpr int ATT_LDS = QL_OFF + 16 * QREC + 256;

__global__ __launch_bounds__(64 * AW) void attn_kernel(const char* __restrict__ ws_ro, char* __restrict__ ws, const char* __restrict__ woa, const char* __restrict__ wob) {
  extern __shared__ __attribute__((aligned(16))) char lds[];
  PROF_BEGIN(2)
  const int tid = threadIdx.x, lane = tid & 63, r = lane & 31, h = lane >> 5;
  const int wid = __builtin_amdgcn_readfirstlane(tid >> 6);
  const int wq = wid & 7, kh = wid >> 3;
  const int xg = blockIdx.x & 7, j = blockIdx.x >> 3;
  const int br = j >> 4, head = 4 * xg + ((j >> 2) & 3), rb = j & 3;
  const char* kv = ws_ro + (br ? WS_KVB : WS_KVA + (size_t)head * NT * REC);
  const char* qp = ws_ro + WS_Q + ((size_t)(br * H + head) * NT + rb * 16) * QREC;
  const unsigned lds0 = (unsigned)(uintptr_t)lds;
  volatile LDSP int* flag = (volatile LDSP int*)(lds0 + NBUF * CH);
  const char* dsrc = kv + wid * REC;
  const unsigned ddst = lds0 + wid * REC;
#define DMA_CHUNK(c) do { const char* s_ = dsrc + (size_t)(c) * CH; const unsigned d_ = rfl(ddst + ((c) % NBUF) * CH); \
    glds16s(s_, lane * 16, d_); glds16s(s_ + 1024, lane * 16, d_ + 1024); if (lane < 41) glds16s(s_ + 2048, lane * 16, d_ + 2048); } while (0)

  const int rm = r <= 21 ? r : 21;
  const unsigned ak0 = h * 512 + r * 16, ak1 = h ? 1280 : 1024 + r * 8, av = VOFF + (h * RB + rm) * 16;

  if (KS == 1) { glds16s(qp + wid * 2560, lane * 16, rfl(lds0 + QL_OFF + wid * 2560)); glds16s(qp + wid * 2560 + 1024, lane * 16, rfl(lds0 + QL_OFF + wid * 2560 + 1024));
                 if (lane < 32) glds16s(qp + wid * 2560 + 2048, lane * 16, rfl(lds0 + QL_OFF + wid * 2560 + 2048)); }
  else { glds16s(qp + wid * 1280, lane * 16, rfl(lds0 + QL_OFF + wid * 1280)); if (lane < 16) glds16s(qp + wid * 1280 + 1024, lane * 16, rfl(lds0 + QL_OFF + wid * 1280 + 1024)); }
  if (tid == 0) *flag = 0;
  DMA_CHUNK(0); DMA_CHUNK(1);
  if (NBUF == 4) DMA_CHUNK(2);
  bf16x8 qa0, qb0; bf16x4 qa1, qb1, qa1d, qb1d;
  f32x16 oa, ob;
  for (int attempt = 0; attempt < 2; ++attempt) {
    if (NBUF == 4) asm volatile("s_waitcnt vmcnt(6) lgkmcnt(0)\n\ts_barrier" ::: "memory");
    else asm volatile("s_waitcnt vmcnt(3) lgkmcnt(0)\n\ts_barrier" ::: "memory");
    {
      const unsigned ql = lds0 + QL_OFF + wq * 2560;
      qa0 = *(const LDSP bf16x8*)(ql + h * 512 + r * 16); qa1d = *(const LDSP bf16x4*)(ql + 1024 + r * 8);
      qb0 = *(const LDSP bf16x8*)(ql + 1280 + h * 512 + r * 16); qb1d = *(const LDSP bf16x4*)(ql + 1280 + 1024 + r * 8);
      const bf16x4 z4 = {0, 0, 0, 0};
      const bf16x4 qaz = h ? z4 : qa1d, qbz = h ? z4 : qb1d;
      float ma, mb;
      if (attempt == 0) {
        const bf16x8 kf0 = *(const LDSP bf16x8*)(lds0 + ak0); const bf16x4 kf1 = *(const LDSP bf16x4*)(lds0 + ak1);
        f32x16 s = MFMA16(kf0, qa0, f32x16{}); s = MFMA8(kf1, qaz, s); ma = xhalf_max(rowmax16(s));
        s = MFMA16(kf0, qb0, f32x16{}); s = MFMA8(kf1, qbz, s); mb = xhalf_max(rowmax16(s));
      } else {
        ma = -INFINITY; mb = -INFINITY;
        for (int t = 0; t < NT; ++t) {
          const char* rec = kv + (size_t)t * REC;
          const bf16x8 kf0 = *(const bf16x8*)(rec + ak0); const bf16x4 kf1 = *(const bf16x4*)(rec + ak1);
          f32x16 s = MFMA16(kf0, qa0, f32x16{}); s = MFMA8(kf1, qaz, s); ma = __builtin_fmaxf(ma, rowmax16(s));
          s = MFMA16(kf0, qb0, f32x16{}); s = MFMA8(kf1, qbz, s); mb = __builtin_fmaxf(mb, rowmax16(s));
        }
        ma = xhalf_max(ma); mb = xhalf_max(mb);
      }
      qa1 = h ? negm_frag(ma) : qa1d; qb1 = h ? negm_frag(mb) : qb1d;
    }
    oa = f32x16{}; ob = f32x16{};
    PROF_MARK1(2)
    f32x16 sa, sb; bf16x8 k0, v0, v1; bf16x4 k1; u32x4 pa0, pa1, pb0, pb1;
    const unsigned tb = lds0 + kh * TPC * REC;
    {
      if (NBUF == 4) asm volatile("s_waitcnt vmcnt(3)\n\ts_barrier" ::: "memory");
      else asm volatile("s_waitcnt vmcnt(0)\n\ts_barrier" ::: "memory");
      k0 = *(const LDSP bf16x8*)(tb + ak0); k1 = *(const LDSP bf16x4*)(tb + ak1);
      sa = MFMA16(k0, qa0, f32x16{}); sa = MFMA8(k1, qa1, sa);
      v0 = *(const LDSP bf16x8*)(tb + av); v1 = v0;
      pb0 = u32x4{0, 0, 0, 0}; pb1 = pb0;
    }
#define EX(x) __builtin_amdgcn_exp2f(x)
#define EXP4(SC, i, P, k) do { const float e0 = EX(SC[i]), e1 = EX(SC[i + 1]), e2 = EX(SC[i + 2]), e3 = EX(SC[i + 3]); P[k] = cvtpk(e0, e1); P[k + 1] = cvtpk(e2, e3); PIN(P); SB(); } while (0)
#define BF(x) __builtin_bit_cast(bf16x8, x)
#define STEP(KN0, KN1, VC) do { \
      sb = MFMA16(k0, qb0, f32x16{}); SB(); k0 = *(const LDSP bf16x8*)(KN0);            EXP4(sa, 0, pa0, 0); \
      ob = MFMA16(v0, BF(pb0), ob); SB(); v0 = *(const LDSP bf16x8*)(VC);               EXP4(sa, 4, pa0, 2); \
      sb = MFMA8(k1, qb1, sb); SB(); k1 = *(const LDSP bf16x4*)(KN1);                   EXP4(sa, 8, pa1, 0); \
      ob = MFMA16(v1, BF(pb1), ob); SB(); v1 = *(const LDSP bf16x8*)((VC) + VS);        EXP4(sa, 12, pa1, 2); \
      sa = MFMA16(k0, qa0, f32x16{}); SB();                                             EXP4(sb, 0, pb0, 0); \
      oa = MFMA16(v0, BF(pa0), oa); SB();                                               EXP4(sb, 4, pb0, 2); \
      sa = MFMA8(k1, qa1, sa); SB();                                                    EXP4(sb, 8, pb1, 0); \
      oa = MFMA16(v1, BF(pa1), oa); SB();                                               EXP4(sb, 12, pb1, 2); \
    } while (0)
    for (int c = 0; c < NCH; ++c) {
      if (c > 0) {
        if (NBUF == 4 && c < NCH - 2) asm volatile("s_waitcnt vmcnt(3) lgkmcnt(0)\n\ts_barrier" ::: "memory");
        else asm volatile("s_waitcnt vmcnt(0) lgkmcnt(0)\n\ts_barrier" ::: "memory");
      }
      if (c + NBUF - 1 < NCH) DMA_CHUNK(c + NBUF - 1);
      if (c == 1 && wid == AW - 1 && lane < 50) { const int li = blockIdx.x * 50 + lane; l2touch((li < 6400 ? woa : wob - 6400 * 128) + (size_t)li * 128, rfl(lds0 + QL_OFF + 16 * QREC)); }
      const unsigned base = tb + (c % NBUF) * CH;
      const unsigned basen = tb + ((c + 1) % NBUF) * CH;
      const LDSP char* pk0 = (const LDSP char*)(base + ak0);
      const LDSP char* pk1 = (const LDSP char*)(base + ak1);
      const LDSP char* pv = (const LDSP char*)(base + av);
      const LDSP char* nk0 = (const LDSP char*)(basen + ak0);
      const LDSP char* nk1 = (const LDSP char*)(basen + ak1);
#pragma unroll
      for (int t = 0; t < TPC; ++t)
        STEP(t + 1 < TPC ? pk0 + (t + 1) * REC : nk0, t + 1 < TPC ? pk1 + (t + 1) * REC : nk1, pv + t * REC);
    }
    ob = MFMA16(v0, BF(pb0), ob); ob = MFMA16(v1, BF(pb1), ob);
    PROF_MARK2(2)
    if (KS == 2) {
      asm volatile("s_waitcnt vmcnt(0) lgkmcnt(0)\n\ts_barrier" ::: "memory");
      const unsigned red = lds0 + wq * 8192 + lane * 16;
      if (kh == 1) {
#pragma unroll
        for (int qd = 0; qd < 3; ++qd) { *(LDSP f32x4*)(red + qd * 1024) = f32x4{oa[4 * qd], oa[4 * qd + 1], oa[4 * qd + 2], oa[4 * qd + 3]};
                                         *(LDSP f32x4*)(red + 4096 + qd * 1024) = f32x4{ob[4 * qd], ob[4 * qd + 1], ob[4 * qd + 2], ob[4 * qd + 3]}; }
      }
      asm volatile("s_waitcnt lgkmcnt(0)\n\ts_barrier" ::: "memory");
      if (kh == 0) {
#pragma unroll
        for (int qd = 0; qd < 3; ++qd) { const f32x4 ta = *(const LDSP f32x4*)(red + qd * 1024), tb2 = *(const LDSP f32x4*)(red + 4096 + qd * 1024);
#pragma unroll
          for (int e = 0; e < 4; ++e) { oa[4 * qd + e] += ta[e]; ob[4 * qd + e] += tb2[e]; } }
      }
    }
    bool bad = false;
    if (kh == 0) {
#pragma unroll
      for (int qb = 0; qb < 2; ++qb) {
        const f32x16& o = qb ? ob : oa;
        auto rr = __builtin_amdgcn_permlane32_swap(__float_as_uint(o[8]), __float_as_uint(o[8]), false, false);
        const float l = __uint_as_float(rr[1]);
        bad |= !(l <= 1.099511627776e12f);
        const float inv = 1.0f / l;
        u16* op = (u16*)(ws + WS_O) + ((size_t)br * S + rb * 512 + wq * 64 + qb * 32 + r) * D + head * DH;
        *(u32x2*)(op + 4 * h) = u32x2{cvtpk(o[0] * inv, o[1] * inv), cvtpk(o[2] * inv, o[3] * inv)};
        *(u32x2*)(op + 8 + 4 * h) = u32x2{cvtpk(o[4] * inv, o[5] * inv), cvtpk(o[6] * inv, o[7] * inv)};
        if (h == 0) *(u32x2*)(op + 16) = u32x2{cvtpk(o[8] * inv, o[9] * inv), cvtpk(o[10] * inv, o[11] * inv)};
      }
    }
    if (attempt == 1) break;
    if (__any(bad)) *flag = 1;
    asm volatile("s_waitcnt lgkmcnt(0)\n\ts_barrier" ::: "memory");
    if (*flag == 0) break;
    asm volatile("s_waitcnt vmcnt(0) lgkmcnt(0)\n\ts_barrier" ::: "memory");
    DMA_CHUNK(0); DMA_CHUNK(1);
    if (NBUF == 4) DMA_CHUNK(2);
  }
  PROF_END(2, ws)
#undef DMA_CHUNK
}

extern "C" void kernel_launch(void* const* d_in, const int* in_sizes, int n_in, void* d_out, int out_size, void* d_ws, size_t ws_size, hipStream_t stream) {
  const u16* xa = (const u16*)d_in[0]; const u16* wqkv = (const u16*)d_in[1]; const u16* woa = (const u16*)d_in[2];
  const u16* xb = (const u16*)d_in[3]; const u16* wq = (const u16*)d_in[4]; const u16* wk = (const u16*)d_in[5];
  const u16* wv = (const u16*)d_in[6]; const u16* wob = (const u16*)d_in[7];
  char* ws = (char*)d_ws;
  (void)hipFuncSetAttribute((const void*)proj_kernel, hipFuncAttributeMaxDynamicSharedMemorySize, PROJ_LDS);
  (void)hipFuncSetAttribute((const void*)attn_kernel, hipFuncAttributeMaxDynamicSharedMemorySize, ATT_LDS);
  (void)hipFuncSetAttribute((const void*)oproj_kernel, hipFuncAttributeMaxDynamicSharedMemorySize, OPROJ_LDS);
  proj_kernel<<<256, 512, PROJ_LDS, stream>>>(xa, wqkv, xb, wq, wk, wv, ws);
  attn_kernel<<<256, 64 * AW, ATT_LDS, stream>>>(ws, ws, (const char*)woa, (const char*)wob);
  oproj_kernel<<<256, 512, OPROJ_LDS, stream>>>((const u16*)(ws + WS_O), woa, wob, (u16*)d_out);
}
```

```cpp
#include <hip/hip_runtime.h>
#include <hip/hip_bf16.h>
#include <stdint.h>

typedef unsigned short u16;
using bf16x8 = __attribute__((ext_vector_type(8))) short;
using bf16x4 = __attribute__((ext_vector_type(4))) short;
using f32x16 = __attribute__((ext_vector_type(16))) float;
using u32x4 = __attribute__((ext_vector_type(4))) unsigned;
using u32x2 = __attribute__((ext_vector_type(2))) unsigned;
using f32x4 = __attribute__((ext_vector_type(4))) float;
typedef float f32x2 __attribute__((ext_vector_type(2)));
typedef __bf16 bfv2 __attribute__((ext_vector_type(2)));
typedef short v4i16_t __attribute__((ext_vector_type(4)));
#define DI __device__ __forceinline__
#define LDSP __attribute__((address_space(3)))

constexpr int S = 2048, D = 640, H = 32, DH = 20;
constexpr int NT = S / 32;
constexpr int RB = 22;
constexpr int VOFF = 1296;
constexpr int VS = 2 * RB * 16;
constexpr int REC = VOFF + 4 * RB * 16;
constexpr int QREC = 1280;
constexpr size_t WS_KVA = 0;
constexpr size_t WS_KVB = WS_KVA + (size_t)H * NT * REC;
constexpr size_t WS_Q = WS_KVB + (size_t)NT * REC + 4096;
constexpr size_t WS_O = WS_Q + (size_t)2 * H * NT * QREC + 4096;
constexpr int OREC = 2560;
constexpr size_t WS_END = WS_O + (size_t)2 * 16 * NT * OREC;
constexpr float QSCALE = 0.22360679774997896f * 1.4426950408889634f;

DI unsigned cvtpk(float lo, float hi) { f32x2 v = {lo, hi}; bfv2 b = __builtin_convertvector(v, bfv2); return __builtin_bit_cast(unsigned, b); }
DI void glds16(const void* g, unsigned lds_base) {
  unsigned sv;
  asm volatile("s_mov_b32 %0, m0\n\ts_mov_b32 m0, %2\n\ts_nop 0\n\tglobal_load_lds_dwordx4 %1, off\n\ts_mov_b32 m0, %0" : "=&s"(sv) : "v"(g), "s"(lds_base) : "memory");
}
DI void glds16s(const void* sbase, unsigned voff, unsigned lds_base) {
  unsigned sv;
  asm volatile("s_mov_b32 %0, m0\n\ts_mov_b32 m0, %3\n\ts_nop 0\n\tglobal_load_lds_dwordx4 %1, %2\n\ts_mov_b32 m0, %0" : "=&s"(sv) : "v"(voff), "s"(sbase), "s"(lds_base) : "memory");
}
DI void l2touch(const void* g, unsigned lds_dummy) {
  unsigned sv;
  asm volatile("s_mov_b32 %0, m0\n\ts_mov_b32 m0, %2\n\ts_nop 0\n\tglobal_load_lds_dword %1, off\n\ts_mov_b32 m0, %0" : "=&s"(sv) : "v"(g), "s"(lds_dummy) : "memory");
}
#define WT1 1
#define WT2 0
#define WT3 0
template <int W> DI void st16(void* p, u32x4 v) {
  if (W == 1) asm volatile("global_store_dwordx4 %0, %1, off sc1\n\ts_nop 1" :: "v"(p), "v"(v) : "memory");
  else *(u32x4*)p = v;
}
template <int W> DI void st8(void* p, u32x2 v) {
  if (W == 1) asm volatile("global_store_dwordx2 %0, %1, off sc1" :: "v"(p), "v"(v) : "memory");
  else *(u32x2*)p = v;
}
#define MFMA16(a, b, c) __builtin_amdgcn_mfma_f32_32x32x16_bf16(a, b, c, 0, 0, 0)
#define MFMA8(a, b, c) __builtin_amdgcn_mfma_f32_32x32x8bf16_1k(a, b, c, 0, 0, 0)
#define SB() __builtin_amdgcn_sched_barrier(0)
#define PIN(x) asm volatile("" : "+v"(x))
DI unsigned rfl(unsigned x) { return (unsigned)__builtin_amdgcn_readfirstlane((int)x); }
DI bf16x4 trread(unsigned a) { return __builtin_bit_cast(bf16x4, __builtin_amdgcn_ds_read_tr16_b64_v4i16((LDSP v4i16_t*)a)); }

constexpr int BK = 64, NKC = D / BK, NST = 4;
constexpr int WROW = 320;
constexpr int WBYTES = BK * WROW;
constexpr int EBYTES = 3072;
DI int colmap_head(int ct, int g) {
  const int o = g >> 2, hi = g & 1, u = (g >> 1) & 1;
  return ct < 4 ? 20 * (ct + 4 * o) + 8 * hi + 4 * u : 20 * (4 * o + 2 * hi + u) + 16;
}
DI int colmap_oct(int ct, int g) { return 32 * ct + 16 * (g >> 2) + 8 * (g & 1) + 4 * ((g >> 1) & 1); }
DI int colmap_id(int ct, int g) { return 32 * ct + 4 * g; }

#define PROF_BEGIN(k)
#define PROF_MARK1(k)
#define PROF_MARK2(k)
#define PROF_END(k, wsp)
#define WAITV(n) asm volatile("s_waitcnt vmcnt(" #n ")" ::: "memory")
#define BARRIER() asm volatile("s_waitcnt lgkmcnt(0)\n\ts_barrier" ::: "memory")

template <int NK, int NA, int NSTG, int STG, bool V04, bool V5, bool ODD, bool XPL, class Mark>
DI void gemm_consume(f32x16 (&acc)[6], const unsigned lds0, const unsigned xa, const unsigned (&wa)[4], const int ks0, Mark&& mark) {
  bf16x8 xf[2][2], wf[2][4];
#define XADDR(ks) (XPL ? xa + (ks) * 2048 : (xa ^ ((ks) << 5)))
#define RDX(set, sb, ks) do { xf[set][0] = *(const LDSP bf16x8*)((sb) + XADDR(ks)); xf[set][1] = *(const LDSP bf16x8*)((sb) + (XPL ? 512 : 4096) + XADDR(ks)); } while (0)
#define RDW(set, sb, ks, i) do { const unsigned rs_ = (i) == 3 ? 40 : WROW; const unsigned a_ = (sb) + wa[i] + (16 * (ks)) * rs_; \
      const bf16x4 lo_ = trread(a_), hi_ = trread(a_ + 4 * rs_); wf[set][i] = __builtin_shufflevector(lo_, hi_, 0, 1, 2, 3, 4, 5, 6, 7); } while (0)
#define MM1(i, X, W, V) acc[i] = (V) ? MFMA16(X, W, acc[i]) : MFMA16(W, X, acc[i])
#define STEPG(cur, sb, ks) do { \
    MM1(0, xf[cur][0], wf[cur][0], V04); SB(); RDX((cur) ^ 1, sb, ks); SB(); \
    MM1(1, xf[cur][1], wf[cur][0], V04); SB(); RDW((cur) ^ 1, sb, ks, 0); SB(); \
    MM1(2, xf[cur][0], wf[cur][1], V04); SB(); RDW((cur) ^ 1, sb, ks, 1); SB(); \
    MM1(3, xf[cur][1], wf[cur][1], V04); SB(); RDW((cur) ^ 1, sb, ks, 2); SB(); \
    MM1(4, xf[cur][ODD ? 1 : 0], wf[cur][2], V04); SB(); if (NA == 6) { RDW((cur) ^ 1, sb, ks, 3); SB(); MM1(5, xf[cur][ODD ? 1 : 0], wf[cur][3], V5); SB(); } } while (0)
  asm volatile("s_barrier" ::: "memory");
  mark();
  RDX(0, lds0, ks0); RDW(0, lds0, ks0, 0); RDW(0, lds0, ks0, 1); RDW(0, lds0, ks0, 2); if (NA == 6) RDW(0, lds0, ks0, 3);
  unsigned sb = lds0; int slot = 0;
#pragma unroll 2
  for (int c = 0; c < NKC; ++c) {
#pragma unroll
    for (int k = 0; k + 1 < NK; ++k) STEPG(k & 1, sb, ks0 + k + 1);
    asm volatile("s_waitcnt lgkmcnt(0)\n\ts_barrier" ::: "memory");
    slot = slot + 1 == NSTG ? 0 : slot + 1; sb = lds0 + slot * STG;
    STEPG(1, sb, ks0);
  }
#undef RDX
#undef XADDR
#undef RDW
#undef MM1
#undef STEPG
}
template <int NSTG, int STG, int NI, class Issue>
DI void gemm_load(const unsigned lds0, Issue&& issue) {
  issue(0, lds0); issue(1, lds0 + STG);
  asm volatile("s_waitcnt vmcnt(%0)" :: "n"(NI) : "memory");
  asm volatile("s_barrier" ::: "memory");
#pragma unroll
  for (int c = 2; c < NSTG; ++c) issue(c, lds0 + c * STG);
  unsigned sb = lds0; int slot = 0;
#pragma unroll
  for (int c = 0; c < NKC; ++c) {
    const int rem = NKC - 2 - c;
    if (rem >= NSTG - 2) asm volatile("s_waitcnt vmcnt(%0)\n\ts_barrier" :: "n"((NSTG - 2) * NI) : "memory");
    else if (NSTG > 3 && rem == NSTG - 3) asm volatile("s_waitcnt vmcnt(%0)\n\ts_barrier" :: "n"((NSTG - 3) * NI) : "memory");
    else if (NSTG > 4 && rem == NSTG - 4) asm volatile("s_waitcnt vmcnt(%0)\n\ts_barrier" :: "n"(NSTG > 4 ? (NSTG - 4) * NI : 0) : "memory");
    else asm volatile("s_waitcnt vmcnt(0)\n\ts_barrier" ::: "memory");
    if (c + NSTG < NKC) issue(c + NSTG, sb);
    slot = slot + 1 == NSTG ? 0 : slot + 1; sb = lds0 + slot * STG;
  }
}

constexpr int PXB = 128 * 128, PSTG = PXB + WBYTES + EBYTES;
template <bool EX>
DI void proj_load(const char* __restrict__ xs, const char* __restrict__ w, const unsigned ldw2, const char* __restrict__ we, const int koff, const unsigned lds0, const int lw, const int lane) {
  const unsigned xe = (32 * lw + (lane >> 3)) * (D * 2) + (((lane & 7) ^ (lane >> 4)) << 4), xod = xe ^ 64;
  unsigned wo[5];
  { const unsigned q = (lane * 205) >> 12; unsigned c = lane - 20 * q, o = (16 * lw + q) * ldw2 + c * 16;
#pragma unroll
    for (int i = 0; i < 5; ++i) { wo[i] = o; c += 4; const bool cy = c >= 20; c = cy ? c - 20 : c; o += 3 * ldw2 + 64 + (cy ? ldw2 - 320 : 0); } }
  const int ep = lw < 2 ? lw : 2;
  const unsigned eo = ep * 1024 + (ep == 2 ? (lane & 31) : lane) * 16;
  auto issue = [&](int ci, unsigned sb) {
    int c = ci + koff; if (c >= NKC) c -= NKC;
    const char* xc = xs + c * (BK * 2); const char* wc = w + (size_t)c * BK * ldw2;
#pragma unroll
    for (int i = 0; i < 4; ++i) glds16s(xc + i * (8 * D * 2), (i & 1) ? xod : xe, sb + (4 * lw + i) * 1024);
#pragma unroll
    for (int i = 0; i < 5; ++i) glds16s(wc, wo[i], sb + PXB + (5 * lw + i) * 1024);
    if (EX) glds16s(we + c * 2560, eo, sb + PXB + WBYTES + ep * 1024);
  };
  gemm_load<NST, PSTG, EX ? 10 : 9>(lds0, issue);
}
template <int KIND, int EXTRA>
DI void proj_tile(int row0, char* qk_base  , size_t head_stride, int rec, char* e_base, const unsigned lds0, char* wsp,
                  char* cbase, const int cheads) {
  PROF_BEGIN(1)
  constexpr int XBYTES = PXB, STG = PSTG;
  const int tid = threadIdx.x, lane = tid & 63, r = lane & 31, h = lane >> 5;
  const int wid = __builtin_amdgcn_readfirstlane(tid >> 6);
  const int tp = wid >> 1, odd = wid & 1;
  const unsigned xa = tp * 8192 + r * 128 + ((h ^ ((r >> 1) & 7)) << 4);
  const int g = 4 * ((lane >> 4) & 1) + (lane & 3), q = (lane & 15) >> 2;
  unsigned wa[4];
#pragma unroll
  for (int i = 0; i < 3; ++i) { const int ct = i < 2 ? 2 * odd + i : 4; wa[i] = XBYTES + (8 * h + q) * WROW + (KIND == 2 ? colmap_id(ct, g) : colmap_head(ct, g)) * 2; }
  { int col;
    if (EXTRA == 1) { const int w8 = 2 * (g >> 2) + (g & 1), u = (g >> 1) & 1; col = w8 < 2 ? 8 * w8 + 4 * u : (w8 == 2 && u == 0 ? 16 : 0); }
    else col = g < 5 ? 4 * g : 0;
    wa[3] = XBYTES + WBYTES + (8 * h + q) * 40 + col * 2; }
  constexpr int NA = EXTRA != 0 ? 6 : 5;
  f32x16 acc[6];
#pragma unroll
  for (int i = 0; i < 6; ++i) acc[i] = f32x16{};
  auto mark = [&]() { PROF_MARK1(1) };
  if (odd) gemm_consume<4, NA, NST, STG, KIND == 2, EXTRA == 2, true, false>(acc, lds0, xa, wa, 0, mark);
  else gemm_consume<4, NA, NST, STG, KIND == 2, EXTRA == 2, false, false>(acc, lds0, xa, wa, 0, mark);
  PROF_MARK2(1)
#pragma unroll
  for (int i = 0; i < NA; ++i) {
    const bool ex = i == 5;
    const int ct = i < 4 ? 2 * odd + (i >> 1) : 4;
    const int tile = (row0 >> 5) + (i < 4 ? 2 * tp + (i & 1) : wid);
    if (ex ? (EXTRA == 2) : (KIND == 2)) {
      const int c = ex ? r : 32 * ct + r;
      const int hl = ex ? 0 : c / 20, d = ex ? r : c - 20 * hl;
      if (!ex || r < 20) {
        char* rb = (ex ? e_base : qk_base + hl * head_stride) + (size_t)tile * REC + VOFF + (h * RB) * 16;
#pragma unroll
        for (int s = 0; s < 2; ++s) {
          u32x4 v = {cvtpk(acc[i][8 * s], acc[i][8 * s + 1]), cvtpk(acc[i][8 * s + 2], acc[i][8 * s + 3]), cvtpk(acc[i][8 * s + 4], acc[i][8 * s + 5]), cvtpk(acc[i][8 * s + 6], acc[i][8 * s + 7])};
          st16<WT1>(rb + s * VS + d * 16, v);
        }
      }
    } else {
      const bool isq = !ex && KIND == 0;
      const float sc = isq ? QSCALE : 1.0f;
#pragma unroll
      for (int o = 0; o < 2; ++o) {
        u32x4 v = {cvtpk(acc[i][8 * o] * sc, acc[i][8 * o + 1] * sc), cvtpk(acc[i][8 * o + 2] * sc, acc[i][8 * o + 3] * sc),
                   cvtpk(acc[i][8 * o + 4] * sc, acc[i][8 * o + 5] * sc), cvtpk(acc[i][8 * o + 6] * sc, acc[i][8 * o + 7] * sc)};
        if (ex) {
          char* rb = e_base + (size_t)tile * REC;
          if (o == 0) st16<WT1>(rb + h * 512 + r * 16, v);
          else if (h == 0) st8<WT1>(rb + 1024 + r * 8, u32x2{v[0], v[1]});
        } else if (i < 4) {
          st16<WT1>(qk_base + (size_t)(ct + 4 * o) * head_stride + (size_t)tile * rec + h * 512 + r * 16, v);
        } else {
          char* rb = qk_base + (size_t)(4 * o + 2 * h) * head_stride + (size_t)tile * rec;
          st8<WT1>(rb + 1024 + r * 8, u32x2{v[0], v[1]});
          st8<WT1>(rb + head_stride + 1024 + r * 8, u32x2{v[2], v[3]});
        }
      }
    }
  }
  if (KIND == 0 && cbase != nullptr) {
    char* cb = cbase + (size_t)((row0 >> 5) + wid) * REC;
    if ((lane >> 3) < cheads) { const unsigned c1 = (lane & 1) ? 0u : 0x3F803F80u;
      st16<WT1>(cb + (size_t)(lane >> 3) * NT * REC + VOFF + (((lane >> 1) & 3) * RB + 20 + (lane & 1)) * 16, u32x4{c1, c1, c1, c1}); }
    if (lane < cheads) st8<WT1>(cb + (size_t)lane * NT * REC + 1280, u32x2{0x3F803F80u, 0x00003F80u});
  }
  PROF_END(1, wsp)
}
constexpr int PROJ_LDS = NST * (128 * 128 + WBYTES + EBYTES) + 256;

__global__ __launch_bounds__(512) void proj_kernel(const u16* __restrict__ xa, const u16* __restrict__ wqkv, const u16* __restrict__ xb, const u16* __restrict__ wq,
                                                   const u16* __restrict__ wk, const u16* __restrict__ wv, char* __restrict__ ws) {
  extern __shared__ __attribute__((aligned(16))) char lds[];
  const int xg = blockIdx.x & 7, j = blockIdx.x >> 3;
  const int rblk = (xg & 1) * 8 + (j & 7), cq = j >> 3;
  const int row0 = rblk * 128, kind = xg >> 1;
  const unsigned lds0 = (unsigned)(uintptr_t)lds;
  const int wid = __builtin_amdgcn_readfirstlane(threadIdx.x >> 6);
  if (wid >= 4) {
    const bool a = xg < 6;
    const char* xs = (const char*)(a ? xa : xb) + (size_t)row0 * D * 2;
    const char* w = (const char*)(a ? wqkv + kind * 640 : wq) + cq * 320;
    const int koff = ((j & 7) + 3 * cq) % NKC;
    if (!a && cq < 2) proj_load<true>(xs, w, 1280, (const char*)(cq ? wv : wk), koff, lds0, wid - 4, threadIdx.x & 63);
    else proj_load<false>(xs, w, a ? 3840 : 1280, nullptr, koff, lds0, wid - 4, threadIdx.x & 63);
    return;
  }
  char* kva = ws + WS_KVA + (size_t)(cq * 8) * NT * REC;
  if (kind == 1) proj_tile<1, 0>(row0, kva, (size_t)NT * REC, REC, nullptr, lds0, ws, nullptr, 0);
  else if (kind == 2) proj_tile<2, 0>(row0, kva, (size_t)NT * REC, REC, nullptr, lds0, ws, nullptr, 0);
  else {
    char* qb = ws + WS_Q + (size_t)((kind ? H : 0) + cq * 8) * NT * QREC;
    if (kind == 3 && cq == 0) proj_tile<0, 1>(row0, qb, (size_t)NT * QREC, QREC, ws + WS_KVB, lds0, ws, nullptr, 0);
    else if (kind == 3 && cq == 1) proj_tile<0, 2>(row0, qb, (size_t)NT * QREC, QREC, ws + WS_KVB, lds0, ws, nullptr, 0);
    else proj_tile<0, 0>(row0, qb, (size_t)NT * QREC, QREC, nullptr, lds0, ws, kind == 0 ? kva : (cq == 2 ? ws + WS_KVB : nullptr), kind == 0 ? 8 : 1);
  }
}

constexpr int ONST = 4, OBK = 80, ONKC = D / OBK;
constexpr int OXB = 10 * 1024;
constexpr int OSTG = OXB + OBK * WROW;
constexpr int OPROJ_LDS = ONST * OSTG;
template <int NTL>
DI void oproj_consume(f32x16 (&acc)[3], const unsigned lds0, const unsigned xa, const unsigned (&wa)[3]) {
  bf16x8 xf[2], wf[2][3];
#define RDX(set, sb, ks) xf[set] = *(const LDSP bf16x8*)((sb) + xa + (ks) * 2048)
#define RDW(set, sb, ks, i) do { const unsigned a_ = (sb) + wa[i] + (16 * (ks)) * WROW; \
      const bf16x4 lo_ = trread(a_), hi_ = trread(a_ + 4 * WROW); wf[set][i] = __builtin_shufflevector(lo_, hi_, 0, 1, 2, 3, 4, 5, 6, 7); } while (0)
#define STEPG(cur, sb, ks) do { \
    acc[0] = MFMA16(wf[cur][0], xf[cur], acc[0]); SB(); RDX((cur) ^ 1, sb, ks); RDW((cur) ^ 1, sb, ks, 0); SB(); \
    acc[1] = MFMA16(wf[cur][1], xf[cur], acc[1]); SB(); RDW((cur) ^ 1, sb, ks, 1); SB(); \
    if (NTL == 3) { acc[2] = MFMA16(wf[cur][2], xf[cur], acc[2]); SB(); RDW((cur) ^ 1, sb, ks, 2); SB(); } } while (0)
  asm volatile("s_barrier" ::: "memory");
  RDX(0, lds0, 0); RDW(0, lds0, 0, 0); RDW(0, lds0, 0, 1); if (NTL == 3) RDW(0, lds0, 0, 2);
  unsigned sb = lds0; int slot = 0;
#define CHUNK(p) do { \
    STEPG(p, sb, 1); STEPG((p) ^ 1, sb, 2); STEPG(p, sb, 3); STEPG((p) ^ 1, sb, 4); \
    asm volatile("s_waitcnt lgkmcnt(0)\n\ts_barrier" ::: "memory");        \
    slot = slot + 1 == ONST ? 0 : slot + 1; sb = lds0 + slot * OSTG; \
    STEPG(p, sb, 0); } while (0)
#pragma unroll 1
  for (int c = 0; c < ONKC; c += 2) { CHUNK(0); CHUNK(1); }
#undef CHUNK
#undef RDX
#undef RDW
#undef STEPG
}
__global__ __launch_bounds__(512) void oproj_kernel(const u16* __restrict__ o, const u16* __restrict__ woa, const u16* __restrict__ wob, u16* __restrict__ out) {
  extern __shared__ __attribute__((aligned(16))) char lds[];
  PROF_BEGIN(3)
  const int tid = threadIdx.x, lane = tid & 63, r = lane & 31, h = lane >> 5;
  const int wid = __builtin_amdgcn_readfirstlane(tid >> 6);
  const int xg = blockIdx.x & 7, j = blockIdx.x >> 3;
  const int br = xg >> 2, rblk = (xg & 3) * 8 + (j & 7), cq = j >> 3;
  const int row0 = rblk * 64;
  const unsigned lds0 = (unsigned)(uintptr_t)lds;
  if (wid >= 4) {
    const int lw = wid - 4;
    const char* xs = (const char*)o + ((size_t)br * 16 * NT + (row0 >> 5)) * OREC;
    const char* w = (const char*)((br ? wob : woa) + cq * 160);
    unsigned vo[9];
    { const unsigned xl = (lane >> 5) * OREC + (lane & 31) * 16;
      const int q0 = 9 * lw - 10;
      const unsigned lq = (lane * 205) >> 12;
      int kk = (64 * q0 + 640) / 20 - 32 + lq + ((64 * q0 + 640) % 20 + lane - 20 * lq >= 20);
      int c16 = (64 * q0 + 640) % 20 + lane - 20 * lq; c16 = c16 >= 20 ? c16 - 20 : c16;
#pragma unroll
      for (int i = 0; i < 9; ++i) {
        const int q = 9 * lw + i;
        if (q < 10) vo[i] = (q / 5) * (NT * OREC) + (q % 5) * 512 + xl;
        else { const int sl = kk >> 3, ii = kk & 7, e = sl >= 5 ? sl - 5 : sl;
          const int k = (sl >= 5 ? 40 : 0) + (e < 4 ? 20 * (e >> 1) + 8 * (e & 1) + ii : (ii < 4 ? 16 + ii : 32 + ii));
          vo[i] = k * (D * 2) + c16 * 16; }
        if (q < 34) { c16 += 4; const bool cy = c16 >= 20; c16 = cy ? c16 - 20 : c16; kk += cy ? 4 : 3; }
      } }
#define ROT3 0
    const int koff = ROT3 == 1 ? ((j & 7) + 3 * cq) % ONKC : ROT3 == 2 ? 2 * (j & 1) : 0;
    auto issue = [&](int ci, unsigned sb) {
      const int c = (ci + koff) & (ONKC - 1);
      const char* xc = xs + (size_t)c * (2 * NT * OREC); const char* wc = w + (size_t)c * OBK * D * 2;
#pragma unroll
      for (int i = 0; i < 9; ++i) { const int q = 9 * lw + i; glds16s(q < 10 ? xc : wc, vo[i], sb + min(q, 34) * 1024); }
    };
    issue(0, lds0); issue(1, lds0 + OSTG);
    asm volatile("s_waitcnt vmcnt(9)\n\ts_barrier" ::: "memory");
    issue(2, lds0 + 2 * OSTG); issue(3, lds0 + 3 * OSTG);
    unsigned sb = lds0; int slot = 0;
#pragma unroll
    for (int c = 0; c < ONKC; ++c) {
      const int rem = ONKC - 2 - c;
      if (rem >= 2) asm volatile("s_waitcnt vmcnt(18)\n\ts_barrier" ::: "memory");
      else if (rem == 1) asm volatile("s_waitcnt vmcnt(9)\n\ts_barrier" ::: "memory");
      else asm volatile("s_waitcnt vmcnt(0)\n\ts_barrier" ::: "memory");
      if (c + ONST < ONKC) issue(c + ONST, sb);
      slot = slot + 1 == ONST ? 0 : slot + 1; sb = lds0 + slot * OSTG;
    }
    PROF_END(3, (char*)o - WS_O)
    return;
  }
  const int tt = wid & 1, cg = wid >> 1;
  const unsigned xa = h * 1024 + (32 * tt + r) * 16;
  const int g = 4 * ((lane >> 4) & 1) + (lane & 3), q4 = (lane & 15) >> 2;
  unsigned wa[3];
#pragma unroll
  for (int i = 0; i < 3; ++i) wa[i] = OXB + (8 * h + q4) * WROW + colmap_oct(cg * 3 + (i < 2 || cg == 0 ? i : 0), g) * 2;
  f32x16 acc[3];
#pragma unroll
  for (int i = 0; i < 3; ++i) acc[i] = f32x16{};
  PROF_MARK1(3)
  if (cg == 0) oproj_consume<3>(acc, lds0, xa, wa); else oproj_consume<2>(acc, lds0, xa, wa);
  PROF_MARK2(3)
  u16* op = out + (size_t)(row0 + 32 * tt + r) * 1280 + br * 640 + cq * 160 + 96 * cg + 8 * h;
#pragma unroll
  for (int i = 0; i < 3; ++i) {
    if (i == 2 && cg == 1) continue;
#pragma unroll
    for (int o8 = 0; o8 < 2; ++o8) {
      u32x4 v = {cvtpk(acc[i][8 * o8], acc[i][8 * o8 + 1]), cvtpk(acc[i][8 * o8 + 2], acc[i][8 * o8 + 3]), cvtpk(acc[i][8 * o8 + 4], acc[i][8 * o8 + 5]), cvtpk(acc[i][8 * o8 + 6], acc[i][8 * o8 + 7])};
      st16<WT3>(op + 32 * i + 16 * o8, v);
    }
  }
  PROF_END(3, (char*)o - WS_O)
}

#define MX3(a, b, c) __builtin_fmaxf(__builtin_fmaxf((a), (b)), (c))
DI float rowmax16(const f32x16& p) {
  float a = MX3(p[0], p[1], p[2]), b = MX3(p[3], p[4], p[5]);
  a = MX3(a, p[6], p[7]); b = MX3(b, p[8], p[9]); a = MX3(a, p[10], p[11]); b = MX3(b, p[12], p[13]); a = MX3(a, p[14], p[15]);
  return __builtin_fmaxf(a, b);
}
DI float xhalf_max(float m) {
  auto rr = __builtin_amdgcn_permlane32_swap(__float_as_uint(m), __float_as_uint(m), false, false);
  return __builtin_fmaxf(__uint_as_float(rr[0]), __uint_as_float(rr[1]));
}
DI bf16x4 negm_frag(float m) {
  const float n = -m;
  const unsigned h1 = cvtpk(n, n) & 0xffffu; const float r1 = n - __uint_as_float(h1 << 16);
  const unsigned h2 = cvtpk(r1, r1) & 0xffffu; const float r2 = r1 - __uint_as_float(h2 << 16);
  const unsigned h3 = cvtpk(r2, r2) & 0xffffu;
  u32x2 p = {h1 | (h2 << 16), h3};
  return __builtin_bit_cast(bf16x4, p);
}

#define KS 1
#define TPCV 8
constexpr int TPC = TPCV;
constexpr int CTILES = TPC * KS;
constexpr int CH = CTILES * REC;
constexpr int NCH = NT / CTILES;
constexpr int NBUF = (KS == 1 && TPC == 8) ? 4 : 3;
constexpr int AW = 8 * KS;
constexpr int NV = CTILES > AW ? 6 : 3;
constexpr int QL_OFF = NBUF * CH + 64;
constexpr int ATT_LDS = QL_OFF + 16 * QREC + 256;

__global__ __launch_bounds__(64 * AW) void attn_kernel(const char* __restrict__ ws_ro, char* __restrict__ ws, const char* __restrict__ woa, const char* __restrict__ wob) {
  extern __shared__ __attribute__((aligned(16))) char lds[];
  PROF_BEGIN(2)
  const int tid = threadIdx.x, lane = tid & 63, r = lane & 31, h = lane >> 5;
  const int wid = __builtin_amdgcn_readfirstlane(tid >> 6);
  const int wq = wid & 7, kh = wid >> 3;
  const int xg = blockIdx.x & 7, j = blockIdx.x >> 3;
  const int br = j >> 4, head = 4 * xg + ((j >> 2) & 3), rb = j & 3;
  const char* kv = ws_ro + (br ? WS_KVB : WS_KVA + (size_t)head * NT * REC);
  const char* qp = ws_ro + WS_Q + ((size_t)(br * H + head) * NT + rb * 16) * QREC;
  const unsigned lds0 = (unsigned)(uintptr_t)lds;
  volatile LDSP int* flag = (volatile LDSP int*)(lds0 + NBUF * CH);
  const char* dsrc = kv + wid * REC;
  const unsigned ddst = lds0 + wid * REC;
#define DMA_REC(s_, d_) do { glds16s(s_, lane * 16, d_); glds16s((s_) + 1024, lane * 16, (d_) + 1024); if (lane < 41) glds16s((s_) + 2048, lane * 16, (d_) + 2048); } while (0)
#define DMA_CHUNK(c) do { const char* s_ = dsrc + (size_t)(c) * CH; const unsigned d_ = rfl(ddst + ((c) % NBUF) * CH); \
    DMA_REC(s_, d_); if (CTILES > AW) DMA_REC(s_ + AW * REC, d_ + AW * REC); } while (0)

  const int rm = r <= 21 ? r : 21;
  const unsigned ak0 = h * 512 + r * 16, ak1 = h ? 1280 : 1024 + r * 8, av = VOFF + (h * RB + rm) * 16;

  if (KS == 1) { glds16s(qp + wid * 2560, lane * 16, rfl(lds0 + QL_OFF + wid * 2560)); glds16s(qp + wid * 2560 + 1024, lane * 16, rfl(lds0 + QL_OFF + wid * 2560 + 1024));
                 if (lane < 32) glds16s(qp + wid * 2560 + 2048, lane * 16, rfl(lds0 + QL_OFF + wid * 2560 + 2048)); }
  else { glds16s(qp + wid * 1280, lane * 16, rfl(lds0 + QL_OFF + wid * 1280)); if (lane < 16) glds16s(qp + wid * 1280 + 1024, lane * 16, rfl(lds0 + QL_OFF + wid * 1280 + 1024)); }
  if (tid == 0) *flag = 0;
  DMA_CHUNK(0); DMA_CHUNK(1);
  bf16x8 qa0, qb0; bf16x4 qa1, qb1, qa1d, qb1d;
  f32x16 oa, ob;
  for (int attempt = 0; attempt < 2; ++attempt) {
    asm volatile("s_waitcnt vmcnt(%0) lgkmcnt(0)\n\ts_barrier" :: "n"(NV) : "memory");
    if (NBUF == 4) DMA_CHUNK(2);
    f32x16 sa, sb; bf16x8 k0, v0, v1; bf16x4 k1; u32x4 pa0, pa1, pb0, pb1;
    const unsigned tb = lds0 + kh * TPC * REC;
    {
      const unsigned ql = lds0 + QL_OFF + wq * 2560;
      qa0 = *(const LDSP bf16x8*)(ql + h * 512 + r * 16); qa1d = *(const LDSP bf16x4*)(ql + 1024 + r * 8);
      qb0 = *(const LDSP bf16x8*)(ql + 1280 + h * 512 + r * 16); qb1d = *(const LDSP bf16x4*)(ql + 1280 + 1024 + r * 8);
      const bf16x4 z4 = {0, 0, 0, 0};
      const bf16x4 qaz = h ? z4 : qa1d, qbz = h ? z4 : qb1d;
      k0 = *(const LDSP bf16x8*)(tb + ak0); k1 = *(const LDSP bf16x4*)(tb + ak1);
      v0 = *(const LDSP bf16x8*)(tb + av);
      sa = MFMA16(k0, qa0, f32x16{}); sb = MFMA16(k0, qb0, f32x16{});
      sa = MFMA8(k1, qaz, sa); sb = MFMA8(k1, qbz, sb);
      float ma, mb;
      if (attempt == 0) {
        const bf16x8 kf0 = *(const LDSP bf16x8*)(lds0 + ak0); const bf16x4 kf1 = *(const LDSP bf16x4*)(lds0 + ak1);
        if (KS == 1) { ma = rowmax16(sa); mb = rowmax16(sb); }
        else { f32x16 s = MFMA16(kf0, qa0, f32x16{}); s = MFMA8(kf1, qaz, s); ma = rowmax16(s);
               s = MFMA16(kf0, qb0, f32x16{}); s = MFMA8(kf1, qbz, s); mb = rowmax16(s); }
      } else {
        ma = -INFINITY; mb = -INFINITY;
        for (int t = 0; t < NT; ++t) {
          const char* rec = kv + (size_t)t * REC;
          const bf16x8 kf0 = *(const bf16x8*)(rec + ak0); const bf16x4 kf1 = *(const bf16x4*)(rec + ak1);
          f32x16 s = MFMA16(kf0, qa0, f32x16{}); s = MFMA8(kf1, qaz, s); ma = __builtin_fmaxf(ma, rowmax16(s));
          s = MFMA16(kf0, qb0, f32x16{}); s = MFMA8(kf1, qbz, s); mb = __builtin_fmaxf(mb, rowmax16(s));
        }
      }
      ma = xhalf_max(ma); mb = xhalf_max(mb);
#pragma unroll
      for (int i = 0; i < 16; ++i) sa[i] -= ma;
      qa1 = h ? negm_frag(ma) : qa1d; qb1 = h ? negm_frag(mb) : qb1d;
    }
    oa = f32x16{}; ob = f32x16{};
    if (wid >= AW / 2) __builtin_amdgcn_s_setprio(1);
    PROF_MARK1(2)
    v1 = v0; pb0 = u32x4{0, 0, 0, 0}; pb1 = pb0;
#define EX(x) __builtin_amdgcn_exp2f(x)
#define EXP4(SC, i, P, k) do { const float e0 = EX(SC[i]), e1 = EX(SC[i + 1]), e2 = EX(SC[i + 2]), e3 = EX(SC[i + 3]); P[k] = cvtpk(e0, e1); P[k + 1] = cvtpk(e2, e3); PIN(P); SB(); } while (0)
#define BF(x) __builtin_bit_cast(bf16x8, x)

#define STEP(KN0, KN1, VC) do { \
      sb = MFMA16(k0, qb0, f32x16{}); SB(); k0 = *(const LDSP bf16x8*)(KN0);            EXP4(sa, 0, pa0, 0); \
      ob = MFMA16(v0, BF(pb0), ob); SB(); v0 = *(const LDSP bf16x8*)(VC);               EXP4(sa, 4, pa0, 2); \
      sb = MFMA8(k1, qb1, sb); SB(); k1 = *(const LDSP bf16x4*)(KN1);                   EXP4(sa, 8, pa1, 0); \
      ob = MFMA16(v1, BF(pb1), ob); SB(); v1 = *(const LDSP bf16x8*)((VC) + VS);        EXP4(sa, 12, pa1, 2); \
      sa = MFMA16(k0, qa0, f32x16{}); SB();                                             EXP4(sb, 0, pb0, 0); \
      oa = MFMA16(v0, BF(pa0), oa); SB();                                               EXP4(sb, 4, pb0, 2); \
      sa = MFMA8(k1, qa1, sa); SB();                                                    EXP4(sb, 8, pb1, 0); \
      oa = MFMA16(v1, BF(pa1), oa); SB();                                               EXP4(sb, 12, pb1, 2); \
    } while (0)
    for (int c = 0; c < NCH; ++c) {
      const unsigned base = tb + (c % NBUF) * CH;
      const unsigned basen = tb + ((c + 1) % NBUF) * CH;
      const LDSP char* pk0 = (const LDSP char*)(base + ak0);
      const LDSP char* pk1 = (const LDSP char*)(base + ak1);
      const LDSP char* pv = (const LDSP char*)(base + av);
      const LDSP char* nk0 = (const LDSP char*)(basen + ak0);
      const LDSP char* nk1 = (const LDSP char*)(basen + ak1);
#pragma unroll
      for (int t = 0; t < TPC; ++t) {
        if (t == TPC / 2) {
          if (NBUF == 4 && c < NCH - 2) asm volatile("s_waitcnt vmcnt(%0)\n\ts_barrier" :: "n"(NV) : "memory");
          else asm volatile("s_waitcnt vmcnt(0)\n\ts_barrier" ::: "memory");
          if (c + NBUF - 1 < NCH) DMA_CHUNK(c + NBUF - 1);
#define PFWO 2
          if (c >= 1 && c <= 4 && wid == AW - 1 && lane < 50) l2touch((xg >> 2 ? wob : woa) + (size_t)(j * 200 + (c - 1) * 50 + lane) * 128, rfl(lds0 + QL_OFF + 16 * QREC));
        }
        STEP(t + 1 < TPC ? pk0 + (t + 1) * REC : nk0, t + 1 < TPC ? pk1 + (t + 1) * REC : nk1, pv + t * REC);
      }
    }
    ob = MFMA16(v0, BF(pb0), ob); ob = MFMA16(v1, BF(pb1), ob);
    PROF_MARK2(2)
    if (KS == 2) {
      asm volatile("s_waitcnt vmcnt(0) lgkmcnt(0)\n\ts_barrier" ::: "memory");
      const unsigned red = lds0 + wq * 8192 + lane * 16;
      if (kh == 1) {
#pragma unroll
        for (int qd = 0; qd < 3; ++qd) { *(LDSP f32x4*)(red + qd * 1024) = f32x4{oa[4 * qd], oa[4 * qd + 1], oa[4 * qd + 2], oa[4 * qd + 3]};
                                         *(LDSP f32x4*)(red + 4096 + qd * 1024) = f32x4{ob[4 * qd], ob[4 * qd + 1], ob[4 * qd + 2], ob[4 * qd + 3]}; }
      }
      asm volatile("s_waitcnt lgkmcnt(0)\n\ts_barrier" ::: "memory");
      if (kh == 0) {
#pragma unroll
        for (int qd = 0; qd < 3; ++qd) { const f32x4 ta = *(const LDSP f32x4*)(red + qd * 1024), tb2 = *(const LDSP f32x4*)(red + 4096 + qd * 1024);
#pragma unroll
          for (int e = 0; e < 4; ++e) { oa[4 * qd + e] += ta[e]; ob[4 * qd + e] += tb2[e]; } }
      }
    }
    bool bad = false;
    if (kh == 0) {
#pragma unroll
      for (int qb = 0; qb < 2; ++qb) {
        const f32x16& o = qb ? ob : oa;
        auto rr = __builtin_amdgcn_permlane32_swap(__float_as_uint(o[8]), __float_as_uint(o[8]), false, false);
        const float l = __uint_as_float(rr[1]);
        bad |= !(l <= 1.099511627776e12f);
        const float inv = __builtin_amdgcn_rcpf(l);
        char* op = ws + WS_O + (((size_t)br * 16 + (head >> 1)) * NT + rb * 16 + wq * 2 + qb) * OREC + r * 16;
        const auto sx = __builtin_amdgcn_permlane32_swap(cvtpk(o[0] * inv, o[1] * inv), cvtpk(o[4] * inv, o[5] * inv), false, false);
        const auto sy = __builtin_amdgcn_permlane32_swap(cvtpk(o[2] * inv, o[3] * inv), cvtpk(o[6] * inv, o[7] * inv), false, false);
        st16<WT2>(op + ((head & 1) * 2 + h) * 512, u32x4{sx[0], sy[0], sx[1], sy[1]});
        if (h == 0) st8<WT2>(op + 2048 + (head & 1) * 8, u32x2{cvtpk(o[8] * inv, o[9] * inv), cvtpk(o[10] * inv, o[11] * inv)});
      }
    }
    if (attempt == 1) break;
    if (__any(bad)) *flag = 1;
    asm volatile("s_waitcnt lgkmcnt(0)\n\ts_barrier" ::: "memory");
    if (*flag == 0) break;
    asm volatile("s_waitcnt vmcnt(0) lgkmcnt(0)\n\ts_barrier" ::: "memory");
    DMA_CHUNK(0); DMA_CHUNK(1);
  }
  PROF_END(2, ws)
#undef DMA_CHUNK
}

extern "C" void kernel_launch(void* const* d_in, const int* in_sizes, int n_in, void* d_out, int out_size, void* d_ws, size_t ws_size, hipStream_t stream) {
  const u16* xa = (const u16*)d_in[0]; const u16* wqkv = (const u16*)d_in[1]; const u16* woa = (const u16*)d_in[2];
  const u16* xb = (const u16*)d_in[3]; const u16* wq = (const u16*)d_in[4]; const u16* wk = (const u16*)d_in[5];
  const u16* wv = (const u16*)d_in[6]; const u16* wob = (const u16*)d_in[7];
  char* ws = (char*)d_ws;
  (void)hipFuncSetAttribute((const void*)proj_kernel, hipFuncAttributeMaxDynamicSharedMemorySize, PROJ_LDS);
  (void)hipFuncSetAttribute((const void*)attn_kernel, hipFuncAttributeMaxDynamicSharedMemorySize, ATT_LDS);
  (void)hipFuncSetAttribute((const void*)oproj_kernel, hipFuncAttributeMaxDynamicSharedMemorySize, OPROJ_LDS);
  proj_kernel<<<256, 512, PROJ_LDS, stream>>>(xa, wqkv, xb, wq, wk, wv, ws);
  attn_kernel<<<256, 64 * AW, ATT_LDS, stream>>>(ws, ws, (const char*)woa, (const char*)wob);
  oproj_kernel<<<256, 512, OPROJ_LDS, stream>>>((const u16*)(ws + WS_O), woa, wob, (u16*)d_out);
}
```

```cpp
#define PROF 1
#include <stdio.h>
#include <hip/hip_runtime.h>
#include <hip/hip_bf16.h>
#include <stdint.h>

typedef unsigned short u16;
using bf16x8 = __attribute__((ext_vector_type(8))) short;
using bf16x4 = __attribute__((ext_vector_type(4))) short;
using f32x16 = __attribute__((ext_vector_type(16))) float;
using u32x4 = __attribute__((ext_vector_type(4))) unsigned;
using u32x2 = __attribute__((ext_vector_type(2))) unsigned;
using f32x4 = __attribute__((ext_vector_type(4))) float;
typedef float f32x2 __attribute__((ext_vector_type(2)));
typedef __bf16 bfv2 __attribute__((ext_vector_type(2)));
typedef short v4i16_t __attribute__((ext_vector_type(4)));
#define DI __device__ __forceinline__
#define LDSP __attribute__((address_space(3)))

constexpr int S = 2048, D = 640, H = 32, DH = 20;
constexpr int NT = S / 32;
constexpr int RB = 22;
constexpr int VOFF = 1296;
constexpr int VS = 2 * RB * 16;
constexpr int REC = VOFF + 4 * RB * 16;
constexpr int QREC = 1280;
constexpr size_t WS_KVA = 0;
constexpr size_t WS_KVB = WS_KVA + (size_t)H * NT * REC;
constexpr size_t WS_Q = WS_KVB + (size_t)NT * REC + 4096;
constexpr size_t WS_O = WS_Q + (size_t)2 * H * NT * QREC + 4096;
constexpr int OREC = 2560;
constexpr size_t WS_END = WS_O + (size_t)2 * 16 * NT * OREC;
constexpr float QSCALE = 0.22360679774997896f * 1.4426950408889634f;

DI unsigned cvtpk(float lo, float hi) { f32x2 v = {lo, hi}; bfv2 b = __builtin_convertvector(v, bfv2); return __builtin_bit_cast(unsigned, b); }
DI void glds16(const void* g, unsigned lds_base) {
  unsigned sv;
  asm volatile("s_mov_b32 %0, m0\n\ts_mov_b32 m0, %2\n\ts_nop 0\n\tglobal_load_lds_dwordx4 %1, off\n\ts_mov_b32 m0, %0" : "=&s"(sv) : "v"(g), "s"(lds_base) : "memory");
}
DI void glds16s(const void* sbase, unsigned voff, unsigned lds_base) {
  unsigned sv;
  asm volatile("s_mov_b32 %0, m0\n\ts_mov_b32 m0, %3\n\ts_nop 0\n\tglobal_load_lds_dwordx4 %1, %2\n\ts_mov_b32 m0, %0" : "=&s"(sv) : "v"(voff), "s"(sbase), "s"(lds_base) : "memory");
}
DI void l2touch(const void* g, unsigned lds_dummy) {
  unsigned sv;
  asm volatile("s_mov_b32 %0, m0\n\ts_mov_b32 m0, %2\n\ts_nop 0\n\tglobal_load_lds_dword %1, off\n\ts_mov_b32 m0, %0" : "=&s"(sv) : "v"(g), "s"(lds_dummy) : "memory");
}
#define WT1 1
#define WT2 0
#define WT3 0
template <int W> DI void st16(void* p, u32x4 v) {
  if (W == 1) asm volatile("global_store_dwordx4 %0, %1, off sc1\n\ts_nop 1" :: "v"(p), "v"(v) : "memory");
  else *(u32x4*)p = v;
}
template <int W> DI void st8(void* p, u32x2 v) {
  if (W == 1) asm volatile("global_store_dwordx2 %0, %1, off sc1" :: "v"(p), "v"(v) : "memory");
  else *(u32x2*)p = v;
}
#define MFMA16(a, b, c) __builtin_amdgcn_mfma_f32_32x32x16_bf16(a, b, c, 0, 0, 0)
#define MFMA8(a, b, c) __builtin_amdgcn_mfma_f32_32x32x8bf16_1k(a, b, c, 0, 0, 0)
#define SB() __builtin_amdgcn_sched_barrier(0)
#define PIN(x) asm volatile("" : "+v"(x))
DI unsigned rfl(unsigned x) { return (unsigned)__builtin_amdgcn_readfirstlane((int)x); }
DI bf16x4 trread(unsigned a) { return __builtin_bit_cast(bf16x4, __builtin_amdgcn_ds_read_tr16_b64_v4i16((LDSP v4i16_t*)a)); }

constexpr int BK = 64, NKC = D / BK, NST = 4;
constexpr int WROW = 320;
constexpr int WBYTES = BK * WROW;
constexpr int EBYTES = 3072;
DI int colmap_head(int ct, int g) {
  const int o = g >> 2, hi = g & 1, u = (g >> 1) & 1;
  return ct < 4 ? 20 * (ct + 4 * o) + 8 * hi + 4 * u : 20 * (4 * o + 2 * hi + u) + 16;
}
DI int colmap_oct(int ct, int g) { return 32 * ct + 16 * (g >> 2) + 8 * (g & 1) + 4 * ((g >> 1) & 1); }
DI int colmap_id(int ct, int g) { return 32 * ct + 4 * g; }

#define PROF_BEGIN(k) unsigned long long pt0 = __builtin_amdgcn_s_memrealtime(), pc0 = __builtin_readcyclecounter(), pc1 = 0, pc2 = 0;
#define PROF_MARK1(k) pc1 = __builtin_readcyclecounter();
#define PROF_MARK2(k) pc2 = __builtin_readcyclecounter();
#define PROF_END(k, wsp) if ((threadIdx.x & 63) == 0) { unsigned long long* pb = (unsigned long long*)((char*)(wsp) + (64u << 20) + ((size_t)k << 20)) + (blockIdx.x * 16 + (threadIdx.x >> 6)) * 8; \
    pb[0] = pt0; pb[1] = __builtin_amdgcn_s_memrealtime(); pb[2] = pc1 - pc0; pb[3] = pc2 - pc1; pb[4] = __builtin_readcyclecounter() - pc2; }
#define WAITV(n) asm volatile("s_waitcnt vmcnt(" #n ")" ::: "memory")
#define BARRIER() asm volatile("s_waitcnt lgkmcnt(0)\n\ts_barrier" ::: "memory")

template <int NK, int NA, int NSTG, int STG, bool V04, bool V5, bool ODD, bool XPL, class Mark>
DI void gemm_consume(f32x16 (&acc)[6], const unsigned lds0, const unsigned xa, const unsigned (&wa)[4], const int ks0, Mark&& mark) {
  bf16x8 xf[2][2], wf[2][4];
#define XADDR(ks) (XPL ? xa + (ks) * 2048 : (xa ^ ((ks) << 5)))
#define RDX(set, sb, ks) do { xf[set][0] = *(const LDSP bf16x8*)((sb) + XADDR(ks)); xf[set][1] = *(const LDSP bf16x8*)((sb) + (XPL ? 512 : 4096) + XADDR(ks)); } while (0)
#define RDW(set, sb, ks, i) do { const unsigned rs_ = (i) == 3 ? 40 : WROW; const unsigned a_ = (sb) + wa[i] + (16 * (ks)) * rs_; \
      const bf16x4 lo_ = trread(a_), hi_ = trread(a_ + 4 * rs_); wf[set][i] = __builtin_shufflevector(lo_, hi_, 0, 1, 2, 3, 4, 5, 6, 7); } while (0)
#define MM1(i, X, W, V) acc[i] = (V) ? MFMA16(X, W, acc[i]) : MFMA16(W, X, acc[i])
#define STEPG(cur, sb, ks) do { \
    MM1(0, xf[cur][0], wf[cur][0], V04); SB(); RDX((cur) ^ 1, sb, ks); SB(); \
    MM1(1, xf[cur][1], wf[cur][0], V04); SB(); RDW((cur) ^ 1, sb, ks, 0); SB(); \
    MM1(2, xf[cur][0], wf[cur][1], V04); SB(); RDW((cur) ^ 1, sb, ks, 1); SB(); \
    MM1(3, xf[cur][1], wf[cur][1], V04); SB(); RDW((cur) ^ 1, sb, ks, 2); SB(); \
    MM1(4, xf[cur][ODD ? 1 : 0], wf[cur][2], V04); SB(); if (NA == 6) { RDW((cur) ^ 1, sb, ks, 3); SB(); MM1(5, xf[cur][ODD ? 1 : 0], wf[cur][3], V5); SB(); } } while (0)
  asm volatile("s_barrier" ::: "memory");
  mark();
  RDX(0, lds0, ks0); RDW(0, lds0, ks0, 0); RDW(0, lds0, ks0, 1); RDW(0, lds0, ks0, 2); if (NA == 6) RDW(0, lds0, ks0, 3);
  unsigned sb = lds0; int slot = 0;
#pragma unroll 2
  for (int c = 0; c < NKC; ++c) {
#pragma unroll
    for (int k = 0; k + 1 < NK; ++k) STEPG(k & 1, sb, ks0 + k + 1);
    asm volatile("s_waitcnt lgkmcnt(0)\n\ts_barrier" ::: "memory");
    slot = slot + 1 == NSTG ? 0 : slot + 1; sb = lds0 + slot * STG;
    STEPG(1, sb, ks0);
  }
#undef RDX
#undef XADDR
#undef RDW
#undef MM1
#undef STEPG
}
template <int NSTG, int STG, int NI, class Issue>
DI void gemm_load(const unsigned lds0, Issue&& issue) {
  issue(0, lds0); issue(1, lds0 + STG);
  asm volatile("s_waitcnt vmcnt(%0)" :: "n"(NI) : "memory");
  asm volatile("s_barrier" ::: "memory");
#pragma unroll
  for (int c = 2; c < NSTG; ++c) issue(c, lds0 + c * STG);
  unsigned sb = lds0; int slot = 0;
#pragma unroll
  for (int c = 0; c < NKC; ++c) {
    const int rem = NKC - 2 - c;
    if (rem >= NSTG - 2) asm volatile("s_waitcnt vmcnt(%0)\n\ts_barrier" :: "n"((NSTG - 2) * NI) : "memory");
    else if (NSTG > 3 && rem == NSTG - 3) asm volatile("s_waitcnt vmcnt(%0)\n\ts_barrier" :: "n"((NSTG - 3) * NI) : "memory");
    else if (NSTG > 4 && rem == NSTG - 4) asm volatile("s_waitcnt vmcnt(%0)\n\ts_barrier" :: "n"(NSTG > 4 ? (NSTG - 4) * NI : 0) : "memory");
    else asm volatile("s_waitcnt vmcnt(0)\n\ts_barrier" ::: "memory");
    if (c + NSTG < NKC) issue(c + NSTG, sb);
    slot = slot + 1 == NSTG ? 0 : slot + 1; sb = lds0 + slot * STG;
  }
}

constexpr int PXB = 128 * 128, PSTG = PXB + WBYTES + EBYTES;
template <bool EX>
DI void proj_load(const char* __restrict__ xs, const char* __restrict__ w, const unsigned ldw2, const char* __restrict__ we, const int koff, const unsigned lds0, const int lw, const int lane) {
  const unsigned xe = (32 * lw + (lane >> 3)) * (D * 2) + (((lane & 7) ^ (lane >> 4)) << 4), xod = xe ^ 64;
  unsigned wo[5];
  { const unsigned q = (lane * 205) >> 12; unsigned c = lane - 20 * q, o = (16 * lw + q) * ldw2 + c * 16;
#pragma unroll
    for (int i = 0; i < 5; ++i) { wo[i] = o; c += 4; const bool cy = c >= 20; c = cy ? c - 20 : c; o += 3 * ldw2 + 64 + (cy ? ldw2 - 320 : 0); } }
  const int ep = lw < 2 ? lw : 2;
  const unsigned eo = ep * 1024 + (ep == 2 ? (lane & 31) : lane) * 16;
  auto issue = [&](int ci, unsigned sb) {
    int c = ci + koff; if (c >= NKC) c -= NKC;
    const char* xc = xs + c * (BK * 2); const char* wc = w + (size_t)c * BK * ldw2;
#pragma unroll
    for (int i = 0; i < 4; ++i) glds16s(xc + i * (8 * D * 2), (i & 1) ? xod : xe, sb + (4 * lw + i) * 1024);
#pragma unroll
    for (int i = 0; i < 5; ++i) glds16s(wc, wo[i], sb + PXB + (5 * lw + i) * 1024);
    if (EX) glds16s(we + c * 2560, eo, sb + PXB + WBYTES + ep * 1024);
  };
  gemm_load<NST, PSTG, EX ? 10 : 9>(lds0, issue);
}
template <int KIND, int EXTRA>
DI void proj_tile(int row0, char* qk_base  , size_t head_stride, int rec, char* e_base, const unsigned lds0, char* wsp,
                  char* cbase, const int cheads) {
  PROF_BEGIN(1)
  constexpr int XBYTES = PXB, STG = PSTG;
  const int tid = threadIdx.x, lane = tid & 63, r = lane & 31, h = lane >> 5;
  const int wid = __builtin_amdgcn_readfirstlane(tid >> 6);
  const int tp = wid >> 1, odd = wid & 1;
  const unsigned xa = tp * 8192 + r * 128 + ((h ^ ((r >> 1) & 7)) << 4);
  const int g = 4 * ((lane >> 4) & 1) + (lane & 3), q = (lane & 15) >> 2;
  unsigned wa[4];
#pragma unroll
  for (int i = 0; i < 3; ++i) { const int ct = i < 2 ? 2 * odd + i : 4; wa[i] = XBYTES + (8 * h + q) * WROW + (KIND == 2 ? colmap_id(ct, g) : colmap_head(ct, g)) * 2; }
  { int col;
    if (EXTRA == 1) { const int w8 = 2 * (g >> 2) + (g & 1), u = (g >> 1) & 1; col = w8 < 2 ? 8 * w8 + 4 * u : (w8 == 2 && u == 0 ? 16 : 0); }
    else col = g < 5 ? 4 * g : 0;
    wa[3] = XBYTES + WBYTES + (8 * h + q) * 40 + col * 2; }
  constexpr int NA = EXTRA != 0 ? 6 : 5;
  f32x16 acc[6];
#pragma unroll
  for (int i = 0; i < 6; ++i) acc[i] = f32x16{};
  auto mark = [&]() { PROF_MARK1(1) };
  if (odd) gemm_consume<4, NA, NST, STG, KIND == 2, EXTRA == 2, true, false>(acc, lds0, xa, wa, 0, mark);
  else gemm_consume<4, NA, NST, STG, KIND == 2, EXTRA == 2, false, false>(acc, lds0, xa, wa, 0, mark);
  PROF_MARK2(1)
#pragma unroll
  for (int i = 0; i < NA; ++i) {
    const bool ex = i == 5;
    const int ct = i < 4 ? 2 * odd + (i >> 1) : 4;
    const int tile = (row0 >> 5) + (i < 4 ? 2 * tp + (i & 1) : wid);
    if (ex ? (EXTRA == 2) : (KIND == 2)) {
      const int c = ex ? r : 32 * ct + r;
      const int hl = ex ? 0 : c / 20, d = ex ? r : c - 20 * hl;
      if (!ex || r < 20) {
        char* rb = (ex ? e_base : qk_base + hl * head_stride) + (size_t)tile * REC + VOFF + (h * RB) * 16;
#pragma unroll
        for (int s = 0; s < 2; ++s) {
          u32x4 v = {cvtpk(acc[i][8 * s], acc[i][8 * s + 1]), cvtpk(acc[i][8 * s + 2], acc[i][8 * s + 3]), cvtpk(acc[i][8 * s + 4], acc[i][8 * s + 5]), cvtpk(acc[i][8 * s + 6], acc[i][8 * s + 7])};
          st16<WT1>(rb + s * VS + d * 16, v);
        }
      }
    } else {
      const bool isq = !ex && KIND == 0;
      const float sc = isq ? QSCALE : 1.0f;
#pragma unroll
      for (int o = 0; o < 2; ++o) {
        u32x4 v = {cvtpk(acc[i][8 * o] * sc, acc[i][8 * o + 1] * sc), cvtpk(acc[i][8 * o + 2] * sc, acc[i][8 * o + 3] * sc),
                   cvtpk(acc[i][8 * o + 4] * sc, acc[i][8 * o + 5] * sc), cvtpk(acc[i][8 * o + 6] * sc, acc[i][8 * o + 7] * sc)};
        if (ex) {
          char* rb = e_base + (size_t)tile * REC;
          if (o == 0) st16<WT1>(rb + h * 512 + r * 16, v);
          else if (h == 0) st8<WT1>(rb + 1024 + r * 8, u32x2{v[0], v[1]});
        } else if (i < 4) {
          st16<WT1>(qk_base + (size_t)(ct + 4 * o) * head_stride + (size_t)tile * rec + h * 512 + r * 16, v);
        } else {
          char* rb = qk_base + (size_t)(4 * o + 2 * h) * head_stride + (size_t)tile * rec;
          st8<WT1>(rb + 1024 + r * 8, u32x2{v[0], v[1]});
          st8<WT1>(rb + head_stride + 1024 + r * 8, u32x2{v[2], v[3]});
        }
      }
    }
  }
  if (KIND == 0 && cbase != nullptr) {
    char* cb = cbase + (size_t)((row0 >> 5) + wid) * REC;
    if ((lane >> 3) < cheads) { const unsigned c1 = (lane & 1) ? 0u : 0x3F803F80u;
      st16<WT1>(cb + (size_t)(lane >> 3) * NT * REC + VOFF + (((lane >> 1) & 3) * RB + 20 + (lane & 1)) * 16, u32x4{c1, c1, c1, c1}); }
    if (lane < cheads) st8<WT1>(cb + (size_t)lane * NT * REC + 1280, u32x2{0x3F803F80u, 0x00003F80u});
  }
  PROF_END(1, wsp)
}
constexpr int PROJ_LDS = NST * (128 * 128 + WBYTES + EBYTES) + 256;

__global__ __launch_bounds__(512) void proj_kernel(const u16* __restrict__ xa, const u16* __restrict__ wqkv, const u16* __restrict__ xb, const u16* __restrict__ wq,
                                                   const u16* __restrict__ wk, const u16* __restrict__ wv, char* __restrict__ ws) {
  extern __shared__ __attribute__((aligned(16))) char lds[];
  const int xg = blockIdx.x & 7, j = blockIdx.x >> 3;
  const int rblk = (xg & 1) * 8 + (j & 7), cq = j >> 3;
  const int row0 = rblk * 128, kind = xg >> 1;
  const unsigned lds0 = (unsigned)(uintptr_t)lds;
  const int wid = __builtin_amdgcn_readfirstlane(threadIdx.x >> 6);
  if (wid >= 4) {
    const bool a = xg < 6;
    const char* xs = (const char*)(a ? xa : xb) + (size_t)row0 * D * 2;
    const char* w = (const char*)(a ? wqkv + kind * 640 : wq) + cq * 320;
    const int koff = ((j & 7) + 3 * cq) % NKC;
    if (!a && cq < 2) proj_load<true>(xs, w, 1280, (const char*)(cq ? wv : wk), koff, lds0, wid - 4, threadIdx.x & 63);
    else proj_load<false>(xs, w, a ? 3840 : 1280, nullptr, koff, lds0, wid - 4, threadIdx.x & 63);
    return;
  }
  char* kva = ws + WS_KVA + (size_t)(cq * 8) * NT * REC;
  if (kind == 1) proj_tile<1, 0>(row0, kva, (size_t)NT * REC, REC, nullptr, lds0, ws, nullptr, 0);
  else if (kind == 2) proj_tile<2, 0>(row0, kva, (size_t)NT * REC, REC, nullptr, lds0, ws, nullptr, 0);
  else {
    char* qb = ws + WS_Q + (size_t)((kind ? H : 0) + cq * 8) * NT * QREC;
    if (kind == 3 && cq == 0) proj_tile<0, 1>(row0, qb, (size_t)NT * QREC, QREC, ws + WS_KVB, lds0, ws, nullptr, 0);
    else if (kind == 3 && cq == 1) proj_tile<0, 2>(row0, qb, (size_t)NT * QREC, QREC, ws + WS_KVB, lds0, ws, nullptr, 0);
    else proj_tile<0, 0>(row0, qb, (size_t)NT * QREC, QREC, nullptr, lds0, ws, kind == 0 ? kva : (cq == 2 ? ws + WS_KVB : nullptr), kind == 0 ? 8 : 1);
  }
}

constexpr int ONST = 4, OBK = 80, ONKC = D / OBK;
constexpr int OXB = 10 * 1024;
constexpr int OSTG = OXB + OBK * WROW;
constexpr int OPROJ_LDS = ONST * OSTG;
template <int NTL>
DI void oproj_consume(f32x16 (&acc)[3], const unsigned lds0, const unsigned xa, const unsigned (&wa)[3]) {
  bf16x8 xf[2], wf[2][3];
#define RDX(set, sb, ks) xf[set] = *(const LDSP bf16x8*)((sb) + xa + (ks) * 2048)
#define RDW(set, sb, ks, i) do { const unsigned a_ = (sb) + wa[i] + (16 * (ks)) * WROW; \
      const bf16x4 lo_ = trread(a_), hi_ = trread(a_ + 4 * WROW); wf[set][i] = __builtin_shufflevector(lo_, hi_, 0, 1, 2, 3, 4, 5, 6, 7); } while (0)
#define STEPG(cur, sb, ks) do { \
    acc[0] = MFMA16(wf[cur][0], xf[cur], acc[0]); SB(); RDX((cur) ^ 1, sb, ks); RDW((cur) ^ 1, sb, ks, 0); SB(); \
    acc[1] = MFMA16(wf[cur][1], xf[cur], acc[1]); SB(); RDW((cur) ^ 1, sb, ks, 1); SB(); \
    if (NTL == 3) { acc[2] = MFMA16(wf[cur][2], xf[cur], acc[2]); SB(); RDW((cur) ^ 1, sb, ks, 2); SB(); } } while (0)
  asm volatile("s_barrier" ::: "memory");
  RDX(0, lds0, 0); RDW(0, lds0, 0, 0); RDW(0, lds0, 0, 1); if (NTL == 3) RDW(0, lds0, 0, 2);
  unsigned sb = lds0; int slot = 0;
#define CHUNK(p) do { \
    STEPG(p, sb, 1); STEPG((p) ^ 1, sb, 2); STEPG(p, sb, 3); STEPG((p) ^ 1, sb, 4); \
    asm volatile("s_waitcnt lgkmcnt(0)\n\ts_barrier" ::: "memory");        \
    slot = slot + 1 == ONST ? 0 : slot + 1; sb = lds0 + slot * OSTG; \
    STEPG(p, sb, 0); } while (0)
#pragma unroll 1
  for (int c = 0; c < ONKC; c += 2) { CHUNK(0); CHUNK(1); }
#undef CHUNK
#undef RDX
#undef RDW
#undef STEPG
}
__global__ __launch_bounds__(512) void oproj_kernel(const u16* __restrict__ o, const u16* __restrict__ woa, const u16* __restrict__ wob, u16* __restrict__ out) {
  extern __shared__ __attribute__((aligned(16))) char lds[];
  PROF_BEGIN(3)
  const int tid = threadIdx.x, lane = tid & 63, r = lane & 31, h = lane >> 5;
  const int wid = __builtin_amdgcn_readfirstlane(tid >> 6);
  const int xg = blockIdx.x & 7, j = blockIdx.x >> 3;
  const int br = xg >> 2, rblk = (xg & 3) * 8 + (j & 7), cq = j >> 3;
  const int row0 = rblk * 64;
  const unsigned lds0 = (unsigned)(uintptr_t)lds;
  if (wid >= 4) {
    const int lw = wid - 4;
    const char* xs = (const char*)o + ((size_t)br * 16 * NT + (row0 >> 5)) * OREC;
    const char* w = (const char*)((br ? wob : woa) + cq * 160);
    unsigned vo[9];
    { const unsigned xl = (lane >> 5) * OREC + (lane & 31) * 16;
      const int q0 = 9 * lw - 10;
      const unsigned lq = (lane * 205) >> 12;
      int kk = (64 * q0 + 640) / 20 - 32 + lq + ((64 * q0 + 640) % 20 + lane - 20 * lq >= 20);
      int c16 = (64 * q0 + 640) % 20 + lane - 20 * lq; c16 = c16 >= 20 ? c16 - 20 : c16;
#pragma unroll
      for (int i = 0; i < 9; ++i) {
        const int q = 9 * lw + i;
        if (q < 10) vo[i] = (q / 5) * (NT * OREC) + (q % 5) * 512 + xl;
        else { const int sl = kk >> 3, ii = kk & 7, e = sl >= 5 ? sl - 5 : sl;
          const int k = (sl >= 5 ? 40 : 0) + (e < 4 ? 20 * (e >> 1) + 8 * (e & 1) + ii : (ii < 4 ? 16 + ii : 32 + ii));
          vo[i] = k * (D * 2) + c16 * 16; }
        if (q < 34) { c16 += 4; const bool cy = c16 >= 20; c16 = cy ? c16 - 20 : c16; kk += cy ? 4 : 3; }
      } }
#define ROT3 0
    const int koff = ROT3 == 1 ? ((j & 7) + 3 * cq) % ONKC : ROT3 == 2 ? 2 * (j & 1) : 0;
    auto issue = [&](int ci, unsigned sb) {
      const int c = (ci + koff) & (ONKC - 1);
      const char* xc = xs + (size_t)c * (2 * NT * OREC); const char* wc = w + (size_t)c * OBK * D * 2;
#pragma unroll
      for (int i = 0; i < 9; ++i) { const int q = 9 * lw + i; glds16s(q < 10 ? xc : wc, vo[i], sb + min(q, 34) * 1024); }
    };
    issue(0, lds0); issue(1, lds0 + OSTG);
    asm volatile("s_waitcnt vmcnt(9)\n\ts_barrier" ::: "memory");
    issue(2, lds0 + 2 * OSTG); issue(3, lds0 + 3 * OSTG);
    unsigned sb = lds0; int slot = 0;
#pragma unroll
    for (int c = 0; c < ONKC; ++c) {
      const int rem = ONKC - 2 - c;
      if (rem >= 2) asm volatile("s_waitcnt vmcnt(18)\n\ts_barrier" ::: "memory");
      else if (rem == 1) asm volatile("s_waitcnt vmcnt(9)\n\ts_barrier" ::: "memory");
      else asm volatile("s_waitcnt vmcnt(0)\n\ts_barrier" ::: "memory");
      if (c + ONST < ONKC) issue(c + ONST, sb);
      slot = slot + 1 == ONST ? 0 : slot + 1; sb = lds0 + slot * OSTG;
    }
    PROF_END(3, (char*)o - WS_O)
    return;
  }
  const int tt = wid & 1, cg = wid >> 1;
  const unsigned xa = h * 1024 + (32 * tt + r) * 16;
  const int g = 4 * ((lane >> 4) & 1) + (lane & 3), q4 = (lane & 15) >> 2;
  unsigned wa[3];
#pragma unroll
  for (int i = 0; i < 3; ++i) wa[i] = OXB + (8 * h + q4) * WROW + colmap_oct(cg * 3 + (i < 2 || cg == 0 ? i : 0), g) * 2;
  f32x16 acc[3];
#pragma unroll
  for (int i = 0; i < 3; ++i) acc[i] = f32x16{};
  PROF_MARK1(3)
  if (cg == 0) oproj_consume<3>(acc, lds0, xa, wa); else oproj_consume<2>(acc, lds0, xa, wa);
  PROF_MARK2(3)
  u16* op = out + (size_t)(row0 + 32 * tt + r) * 1280 + br * 640 + cq * 160 + 96 * cg + 8 * h;
#pragma unroll
  for (int i = 0; i < 3; ++i) {
    if (i == 2 && cg == 1) continue;
#pragma unroll
    for (int o8 = 0; o8 < 2; ++o8) {
      u32x4 v = {cvtpk(acc[i][8 * o8], acc[i][8 * o8 + 1]), cvtpk(acc[i][8 * o8 + 2], acc[i][8 * o8 + 3]), cvtpk(acc[i][8 * o8 + 4], acc[i][8 * o8 + 5]), cvtpk(acc[i][8 * o8 + 6], acc[i][8 * o8 + 7])};
      st16<WT3>(op + 32 * i + 16 * o8, v);
    }
  }
  PROF_END(3, (char*)o - WS_O)
}

#define MX3(a, b, c) __builtin_fmaxf(__builtin_fmaxf((a), (b)), (c))
DI float rowmax16(const f32x16& p) {
  float a = MX3(p[0], p[1], p[2]), b = MX3(p[3], p[4], p[5]);
  a = MX3(a, p[6], p[7]); b = MX3(b, p[8], p[9]); a = MX3(a, p[10], p[11]); b = MX3(b, p[12], p[13]); a = MX3(a, p[14], p[15]);
  return __builtin_fmaxf(a, b);
}
DI float xhalf_max(float m) {
  auto rr = __builtin_amdgcn_permlane32_swap(__float_as_uint(m), __float_as_uint(m), false, false);
  return __builtin_fmaxf(__uint_as_float(rr[0]), __uint_as_float(rr[1]));
}
DI bf16x4 negm_frag(float m) {
  const float n = -m;
  const unsigned h1 = cvtpk(n, n) & 0xffffu; const float r1 = n - __uint_as_float(h1 << 16);
  const unsigned h2 = cvtpk(r1, r1) & 0xffffu; const float r2 = r1 - __uint_as_float(h2 << 16);
  const unsigned h3 = cvtpk(r2, r2) & 0xffffu;
  u32x2 p = {h1 | (h2 << 16), h3};
  return __builtin_bit_cast(bf16x4, p);
}

#define KS 1
#define TPCV 8
constexpr int TPC = TPCV;
constexpr int CTILES = TPC * KS;
constexpr int CH = CTILES * REC;
constexpr int NCH = NT / CTILES;
constexpr int NBUF = (KS == 1 && TPC == 8) ? 4 : 3;
constexpr int AW = 8 * KS;
constexpr int NV = CTILES > AW ? 6 : 3;
constexpr int QL_OFF = NBUF * CH + 64;
constexpr int ATT_LDS = QL_OFF + 16 * QREC + 256;

__global__ __launch_bounds__(64 * AW) void attn_kernel(const char* __restrict__ ws_ro, char* __restrict__ ws, const char* __restrict__ woa, const char* __restrict__ wob) {
  extern __shared__ __attribute__((aligned(16))) char lds[];
  PROF_BEGIN(2)
  const int tid = threadIdx.x, lane = tid & 63, r = lane & 31, h = lane >> 5;
  const int wid = __builtin_amdgcn_readfirstlane(tid >> 6);
  const int wq = wid & 7, kh = wid >> 3;
  const int xg = blockIdx.x & 7, j = blockIdx.x >> 3;
  const int br = j >> 4, head = 4 * xg + ((j >> 2) & 3), rb = j & 3;
  const char* kv = ws_ro + (br ? WS_KVB : WS_KVA + (size_t)head * NT * REC);
  const char* qp = ws_ro + WS_Q + ((size_t)(br * H + head) * NT + rb * 16) * QREC;
  const unsigned lds0 = (unsigned)(uintptr_t)lds;
  volatile LDSP int* flag = (volatile LDSP int*)(lds0 + NBUF * CH);
  const char* dsrc = kv + wid * REC;
  const unsigned ddst = lds0 + wid * REC;
#define DMA_REC(s_, d_) do { glds16s(s_, lane * 16, d_); glds16s((s_) + 1024, lane * 16, (d_) + 1024); if (lane < 41) glds16s((s_) + 2048, lane * 16, (d_) + 2048); } while (0)
#define DMA_CHUNK(c) do { const char* s_ = dsrc + (size_t)(c) * CH; const unsigned d_ = rfl(ddst + ((c) % NBUF) * CH); \
    DMA_REC(s_, d_); if (CTILES > AW) DMA_REC(s_ + AW * REC, d_ + AW * REC); } while (0)

  const int rm = r <= 21 ? r : 21;
  const unsigned ak0 = h * 512 + r * 16, ak1 = h ? 1280 : 1024 + r * 8, av = VOFF + (h * RB + rm) * 16;

  if (KS == 1) { glds16s(qp + wid * 2560, lane * 16, rfl(lds0 + QL_OFF + wid * 2560)); glds16s(qp + wid * 2560 + 1024, lane * 16, rfl(lds0 + QL_OFF + wid * 2560 + 1024));
                 if (lane < 32) glds16s(qp + wid * 2560 + 2048, lane * 16, rfl(lds0 + QL_OFF + wid * 2560 + 2048)); }
  else { glds16s(qp + wid * 1280, lane * 16, rfl(lds0 + QL_OFF + wid * 1280)); if (lane < 16) glds16s(qp + wid * 1280 + 1024, lane * 16, rfl(lds0 + QL_OFF + wid * 1280 + 1024)); }
  if (tid == 0) *flag = 0;
  DMA_CHUNK(0); DMA_CHUNK(1);
  bf16x8 qa0, qb0; bf16x4 qa1, qb1, qa1d, qb1d;
  f32x16 oa, ob;
  for (int attempt = 0; attempt < 2; ++attempt) {
    asm volatile("s_waitcnt vmcnt(%0) lgkmcnt(0)\n\ts_barrier" :: "n"(NV) : "memory");
    if (NBUF == 4) DMA_CHUNK(2);
    f32x16 sa, sb; bf16x8 k0, v0, v1; bf16x4 k1; u32x4 pa0, pa1, pb0, pb1;
    const unsigned tb = lds0 + kh * TPC * REC;
    {
      const unsigned ql = lds0 + QL_OFF + wq * 2560;
      qa0 = *(const LDSP bf16x8*)(ql + h * 512 + r * 16); qa1d = *(const LDSP bf16x4*)(ql + 1024 + r * 8);
      qb0 = *(const LDSP bf16x8*)(ql + 1280 + h * 512 + r * 16); qb1d = *(const LDSP bf16x4*)(ql + 1280 + 1024 + r * 8);
      const bf16x4 z4 = {0, 0, 0, 0};
      const bf16x4 qaz = h ? z4 : qa1d, qbz = h ? z4 : qb1d;
      k0 = *(const LDSP bf16x8*)(tb + ak0); k1 = *(const LDSP bf16x4*)(tb + ak1);
      v0 = *(const LDSP bf16x8*)(tb + av);
      sa = MFMA16(k0, qa0, f32x16{}); sb = MFMA16(k0, qb0, f32x16{});
      sa = MFMA8(k1, qaz, sa); sb = MFMA8(k1, qbz, sb);
      float ma, mb;
      if (attempt == 0) {
        const bf16x8 kf0 = *(const LDSP bf16x8*)(lds0 + ak0); const bf16x4 kf1 = *(const LDSP bf16x4*)(lds0 + ak1);
        if (KS == 1) { ma = rowmax16(sa); mb = rowmax16(sb); }
        else { f32x16 s = MFMA16(kf0, qa0, f32x16{}); s = MFMA8(kf1, qaz, s); ma = rowmax16(s);
               s = MFMA16(kf0, qb0, f32x16{}); s = MFMA8(kf1, qbz, s); mb = rowmax16(s); }
      } else {
        ma = -INFINITY; mb = -INFINITY;
        for (int t = 0; t < NT; ++t) {
          const char* rec = kv + (size_t)t * REC;
          const bf16x8 kf0 = *(const bf16x8*)(rec + ak0); const bf16x4 kf1 = *(const bf16x4*)(rec + ak1);
          f32x16 s = MFMA16(kf0, qa0, f32x16{}); s = MFMA8(kf1, qaz, s); ma = __builtin_fmaxf(ma, rowmax16(s));
          s = MFMA16(kf0, qb0, f32x16{}); s = MFMA8(kf1, qbz, s); mb = __builtin_fmaxf(mb, rowmax16(s));
        }
      }
      ma = xhalf_max(ma); mb = xhalf_max(mb);
#pragma unroll
      for (int i = 0; i < 16; ++i) sa[i] -= ma;
      qa1 = h ? negm_frag(ma) : qa1d; qb1 = h ? negm_frag(mb) : qb1d;
    }
    oa = f32x16{}; ob = f32x16{};
    if (wid >= AW / 2) __builtin_amdgcn_s_setprio(1);
    PROF_MARK1(2)
    v1 = v0; pb0 = u32x4{0, 0, 0, 0}; pb1 = pb0;
#define EX(x) __builtin_amdgcn_exp2f(x)
#define EXP4(SC, i, P, k) do { const float e0 = EX(SC[i]), e1 = EX(SC[i + 1]), e2 = EX(SC[i + 2]), e3 = EX(SC[i + 3]); P[k] = cvtpk(e0, e1); P[k + 1] = cvtpk(e2, e3); PIN(P); SB(); } while (0)
#define BF(x) __builtin_bit_cast(bf16x8, x)

#define STEP(KN0, KN1, VC) do { \
      sb = MFMA16(k0, qb0, f32x16{}); SB(); k0 = *(const LDSP bf16x8*)(KN0);            EXP4(sa, 0, pa0, 0); \
      ob = MFMA16(v0, BF(pb0), ob); SB(); v0 = *(const LDSP bf16x8*)(VC);               EXP4(sa, 4, pa0, 2); \
      sb = MFMA8(k1, qb1, sb); SB(); k1 = *(const LDSP bf16x4*)(KN1);                   EXP4(sa, 8, pa1, 0); \
      ob = MFMA16(v1, BF(pb1), ob); SB(); v1 = *(const LDSP bf16x8*)((VC) + VS);        EXP4(sa, 12, pa1, 2); \
      sa = MFMA16(k0, qa0, f32x16{}); SB();                                             EXP4(sb, 0, pb0, 0); \
      oa = MFMA16(v0, BF(pa0), oa); SB();                                               EXP4(sb, 4, pb0, 2); \
      sa = MFMA8(k1, qa1, sa); SB();                                                    EXP4(sb, 8, pb1, 0); \
      oa = MFMA16(v1, BF(pa1), oa); SB();                                               EXP4(sb, 12, pb1, 2); \
    } while (0)
    for (int c = 0; c < NCH; ++c) {
      const unsigned base = tb + (c % NBUF) * CH;
      const unsigned basen = tb + ((c + 1) % NBUF) * CH;
      const LDSP char* pk0 = (const LDSP char*)(base + ak0);
      const LDSP char* pk1 = (const LDSP char*)(base + ak1);
      const LDSP char* pv = (const LDSP char*)(base + av);
      const LDSP char* nk0 = (const LDSP char*)(basen + ak0);
      const LDSP char* nk1 = (const LDSP char*)(basen + ak1);
#pragma unroll
      for (int t = 0; t < TPC; ++t) {
        if (t == TPC / 2) {
          if (NBUF == 4 && c < NCH - 2) asm volatile("s_waitcnt vmcnt(%0)\n\ts_barrier" :: "n"(NV) : "memory");
          else asm volatile("s_waitcnt vmcnt(0)\n\ts_barrier" ::: "memory");
          if (c + NBUF - 1 < NCH) DMA_CHUNK(c + NBUF - 1);
#define PFWO 2
          if (c >= 1 && c <= 4 && wid == AW - 1 && lane < 50) l2touch((xg >> 2 ? wob : woa) + (size_t)(j * 200 + (c - 1) * 50 + lane) * 128, rfl(lds0 + QL_OFF + 16 * QREC));
        }
        STEP(t + 1 < TPC ? pk0 + (t + 1) * REC : nk0, t + 1 < TPC ? pk1 + (t + 1) * REC : nk1, pv + t * REC);
      }
    }
    ob = MFMA16(v0, BF(pb0), ob); ob = MFMA16(v1, BF(pb1), ob);
    PROF_MARK2(2)
    if (KS == 2) {
      asm volatile("s_waitcnt vmcnt(0) lgkmcnt(0)\n\ts_barrier" ::: "memory");
      const unsigned red = lds0 + wq * 8192 + lane * 16;
      if (kh == 1) {
#pragma unroll
        for (int qd = 0; qd < 3; ++qd) { *(LDSP f32x4*)(red + qd * 1024) = f32x4{oa[4 * qd], oa[4 * qd + 1], oa[4 * qd + 2], oa[4 * qd + 3]};
                                         *(LDSP f32x4*)(red + 4096 + qd * 1024) = f32x4{ob[4 * qd], ob[4 * qd + 1], ob[4 * qd + 2], ob[4 * qd + 3]}; }
      }
      asm volatile("s_waitcnt lgkmcnt(0)\n\ts_barrier" ::: "memory");
      if (kh == 0) {
#pragma unroll
        for (int qd = 0; qd < 3; ++qd) { const f32x4 ta = *(const LDSP f32x4*)(red + qd * 1024), tb2 = *(const LDSP f32x4*)(red + 4096 + qd * 1024);
#pragma unroll
          for (int e = 0; e < 4; ++e) { oa[4 * qd + e] += ta[e]; ob[4 * qd + e] += tb2[e]; } }
      }
    }
    bool bad = false;
    if (kh == 0) {
#pragma unroll
      for (int qb = 0; qb < 2; ++qb) {
        const f32x16& o = qb ? ob : oa;
        auto rr = __builtin_amdgcn_permlane32_swap(__float_as_uint(o[8]), __float_as_uint(o[8]), false, false);
        const float l = __uint_as_float(rr[1]);
        bad |= !(l <= 1.099511627776e12f);
        const float inv = __builtin_amdgcn_rcpf(l);
        char* op = ws + WS_O + (((size_t)br * 16 + (head >> 1)) * NT + rb * 16 + wq * 2 + qb) * OREC + r * 16;
        const auto sx = __builtin_amdgcn_permlane32_swap(cvtpk(o[0] * inv, o[1] * inv), cvtpk(o[4] * inv, o[5] * inv), false, false);
        const auto sy = __builtin_amdgcn_permlane32_swap(cvtpk(o[2] * inv, o[3] * inv), cvtpk(o[6] * inv, o[7] * inv), false, false);
        st16<WT2>(op + ((head & 1) * 2 + h) * 512, u32x4{sx[0], sy[0], sx[1], sy[1]});
        if (h == 0) st8<WT2>(op + 2048 + (head & 1) * 8, u32x2{cvtpk(o[8] * inv, o[9] * inv), cvtpk(o[10] * inv, o[11] * inv)});
      }
    }
    if (attempt == 1) break;
    if (__any(bad)) *flag = 1;
    asm volatile("s_waitcnt lgkmcnt(0)\n\ts_barrier" ::: "memory");
    if (*flag == 0) break;
    asm volatile("s_waitcnt vmcnt(0) lgkmcnt(0)\n\ts_barrier" ::: "memory");
    DMA_CHUNK(0); DMA_CHUNK(1);
  }
  PROF_END(2, ws)
#undef DMA_CHUNK
}

__global__ void mark_kernel(char* ws, int idx) { if (threadIdx.x == 0) ((unsigned long long*)(ws + (70u << 20)))[idx] = __builtin_amdgcn_s_memrealtime(); }
__global__ void report_kernel(char* ws) {
  if (threadIdx.x != 0) return;
  const unsigned long long tnow = __builtin_amdgcn_s_memrealtime();
  const unsigned long long tA = ((unsigned long long*)(ws + (70u << 20)))[0];
  const unsigned long long tB = ((unsigned long long*)(ws + (70u << 20)))[1];
  const int nw[4] = {0, 4, 8, 4};
  for (int k = 1; k <= 3; ++k) {
    const unsigned long long* pb = (const unsigned long long*)(ws + (64u << 20) + ((size_t)k << 20));
    unsigned long long s0 = ~0ull, s1 = 0, e0 = ~0ull, e1 = 0, pro = 0, mn = 0, ep = 0, cyc = 0, rt = 0;
    for (int b = 0; b < 256; ++b) for (int w = 0; w < nw[k]; ++w) { const unsigned long long* p = pb + (b * 16 + w) * 8;
      s0 = p[0] < s0 ? p[0] : s0; s1 = p[0] > s1 ? p[0] : s1; e0 = p[1] < e0 ? p[1] : e0; e1 = p[1] > e1 ? p[1] : e1; pro += p[2]; mn += p[3]; ep += p[4]; cyc += p[2] + p[3] + p[4]; rt += p[1] - p[0]; }
    const int n = 256 * nw[k];
    printf("K%d start %d..%d end %d..%d (x10ns after marker) avg pro %d main %d epi %d clk %d MHz\n", k, (int)(s0 - tA), (int)(s1 - tA), (int)(e0 - tA), (int)(e1 - tA), (int)(pro / n), (int)(mn / n), (int)(ep / n), (int)(cyc * 100 / rt));
  }
  printf("end marker %d\n", (int)(tB - tA));
}
extern "C" void kernel_launch(void* const* d_in, const int* in_sizes, int n_in, void* d_out, int out_size, void* d_ws, size_t ws_size, hipStream_t stream) {
  const u16* xa = (const u16*)d_in[0]; const u16* wqkv = (const u16*)d_in[1]; const u16* woa = (const u16*)d_in[2];
  const u16* xb = (const u16*)d_in[3]; const u16* wq = (const u16*)d_in[4]; const u16* wk = (const u16*)d_in[5];
  const u16* wv = (const u16*)d_in[6]; const u16* wob = (const u16*)d_in[7];
  char* ws = (char*)d_ws;
  (void)hipFuncSetAttribute((const void*)proj_kernel, hipFuncAttributeMaxDynamicSharedMemorySize, PROJ_LDS);
  (void)hipFuncSetAttribute((const void*)attn_kernel, hipFuncAttributeMaxDynamicSharedMemorySize, ATT_LDS);
  (void)hipFuncSetAttribute((const void*)oproj_kernel, hipFuncAttributeMaxDynamicSharedMemorySize, OPROJ_LDS);
  mark_kernel<<<1, 64, 0, stream>>>(ws, 0);
  proj_kernel<<<256, 512, PROJ_LDS, stream>>>(xa, wqkv, xb, wq, wk, wv, ws);
  attn_kernel<<<256, 64 * AW, ATT_LDS, stream>>>(ws, ws, (const char*)woa, (const char*)wob);
  oproj_kernel<<<256, 512, OPROJ_LDS, stream>>>((const u16*)(ws + WS_O), woa, wob, (u16*)d_out);
  mark_kernel<<<1, 64, 0, stream>>>(ws, 1);
  report_kernel<<<1, 64, 0, stream>>>(ws);
}
```

```cpp
#include <hip/hip_runtime.h>
#include <hip/hip_bf16.h>
#include <stdint.h>
#pragma clang diagnostic ignored "-Wint-to-pointer-cast"

typedef unsigned short u16;
using bf16x8 = __attribute__((ext_vector_type(8))) short;
using bf16x4 = __attribute__((ext_vector_type(4))) short;
using f32x16 = __attribute__((ext_vector_type(16))) float;
using u32x4 = __attribute__((ext_vector_type(4))) unsigned;
using u32x2 = __attribute__((ext_vector_type(2))) unsigned;
using f32x4 = __attribute__((ext_vector_type(4))) float;
typedef float f32x2 __attribute__((ext_vector_type(2)));
typedef __bf16 bfv2 __attribute__((ext_vector_type(2)));
typedef short v4i16_t __attribute__((ext_vector_type(4)));
#define DI __device__ __forceinline__
#define LDSP __attribute__((address_space(3)))

constexpr int S = 2048, D = 640, H = 32, DH = 20;
constexpr int NT = S / 32;
constexpr int RB = 22;
constexpr int VOFF = 1296;
constexpr int VS = 2 * RB * 16;
constexpr int REC = VOFF + 4 * RB * 16;
constexpr int QREC = 1280;
constexpr size_t WS_KVA = 0;
constexpr size_t WS_KVB = WS_KVA + (size_t)H * NT * REC;
constexpr size_t WS_Q = WS_KVB + (size_t)NT * REC + 4096;
constexpr size_t WS_O = WS_Q + (size_t)2 * H * NT * QREC + 4096;
constexpr int OREC = 2560;
constexpr size_t WS_END = WS_O + (size_t)2 * 16 * NT * OREC;
constexpr float QSCALE = 0.22360679774997896f * 1.4426950408889634f;

DI unsigned cvtpk(float lo, float hi) { f32x2 v = {lo, hi}; bfv2 b = __builtin_convertvector(v, bfv2); return __builtin_bit_cast(unsigned, b); }
DI void glds16s(const void* sbase, unsigned voff, unsigned lds_base) {
  unsigned sv;
  asm volatile("s_mov_b32 %0, m0\n\ts_mov_b32 m0, %3\n\ts_nop 2\n\tglobal_load_lds_dwordx4 %1, %2\n\ts_mov_b32 m0, %0" : "=&s"(sv) : "v"(voff), "s"(sbase), "s"(lds_base) : "memory");
}
DI void l2touch(const void* g, unsigned lds_dummy) {
  unsigned sv;
  asm volatile("s_mov_b32 %0, m0\n\ts_mov_b32 m0, %2\n\ts_nop 0\n\tglobal_load_lds_dword %1, off\n\ts_mov_b32 m0, %0" : "=&s"(sv) : "v"(g), "s"(lds_dummy) : "memory");
}
constexpr int WT1 = 1, WT2 = 1, WT3 = 0;
template <int W> DI void st16(void* p, u32x4 v) {
  if (W == 1) asm volatile("global_store_dwordx4 %0, %1, off sc1\n\ts_nop 1" :: "v"(p), "v"(v) : "memory");
  else *(u32x4*)p = v;
}
template <int W> DI void st8(void* p, u32x2 v) {
  if (W == 1) asm volatile("global_store_dwordx2 %0, %1, off sc1" :: "v"(p), "v"(v) : "memory");
  else *(u32x2*)p = v;
}
#define MFMA16(a, b, c) __builtin_amdgcn_mfma_f32_32x32x16_bf16(a, b, c, 0, 0, 0)
#define MFMA8(a, b, c) __builtin_amdgcn_mfma_f32_32x32x8bf16_1k(a, b, c, 0, 0, 0)
#define SB() __builtin_amdgcn_sched_barrier(0)
#define PIN(x) asm volatile("" : "+v"(x))
DI unsigned rfl(unsigned x) { return (unsigned)__builtin_amdgcn_readfirstlane((int)x); }
DI bf16x4 trread(unsigned a) { return __builtin_bit_cast(bf16x4, __builtin_amdgcn_ds_read_tr16_b64_v4i16((LDSP v4i16_t*)a)); }

#define SYM_ADDR(name, out) do { unsigned long long pc_; int d_; \
    asm volatile("s_getpc_b64 %0\n\ts_mov_b32 %1, " name "@rel32@lo+4" : "=s"(pc_), "=s"(d_)); out = (const char*)(pc_ + (long long)d_); } while (0)
constexpr int BK = 64, NKC = D / BK, NST = 4;
constexpr int WROW = 320;
constexpr int WBYTES = BK * WROW;
constexpr int EBYTES = 3072;
DI int colmap_head(int ct, int g) {
  const int o = g >> 2, hi = g & 1, u = (g >> 1) & 1;
  return ct < 4 ? 20 * (ct + 4 * o) + 8 * hi + 4 * u : 20 * (4 * o + 2 * hi + u) + 16;
}
DI int colmap_oct(int ct, int g) { return 32 * ct + 16 * (g >> 2) + 8 * (g & 1) + 4 * ((g >> 1) & 1); }
DI int colmap_id(int ct, int g) { return 32 * ct + 4 * g; }

template <int NA, int STG, bool V04, bool V5, bool ODD>
DI void gemm_consume(f32x16 (&acc)[6], const unsigned lds0, const unsigned xa, const unsigned (&wa)[4]) {
  bf16x8 xf[2][2], wf[2][4];
#define RDX(set, sb, ks) do { xf[set][0] = *(const LDSP bf16x8*)((sb) + (xa ^ ((ks) << 5))); xf[set][1] = *(const LDSP bf16x8*)((sb) + 4096 + (xa ^ ((ks) << 5))); } while (0)
#define RDW(set, sb, ks, i) do { const unsigned rs_ = (i) == 3 ? 40 : WROW; const unsigned a_ = (sb) + wa[i] + (16 * (ks)) * rs_; \
      const bf16x4 lo_ = trread(a_), hi_ = trread(a_ + 4 * rs_); wf[set][i] = __builtin_shufflevector(lo_, hi_, 0, 1, 2, 3, 4, 5, 6, 7); } while (0)
#define MM1(i, X, W, V) acc[i] = (V) ? MFMA16(X, W, acc[i]) : MFMA16(W, X, acc[i])
#define STEPG(cur, sb, ks) do { \
    MM1(0, xf[cur][0], wf[cur][0], V04); SB(); RDX((cur) ^ 1, sb, ks); SB(); \
    MM1(1, xf[cur][1], wf[cur][0], V04); SB(); RDW((cur) ^ 1, sb, ks, 0); SB(); \
    MM1(2, xf[cur][0], wf[cur][1], V04); SB(); RDW((cur) ^ 1, sb, ks, 1); SB(); \
    MM1(3, xf[cur][1], wf[cur][1], V04); SB(); RDW((cur) ^ 1, sb, ks, 2); SB(); \
    MM1(4, xf[cur][ODD ? 1 : 0], wf[cur][2], V04); SB(); if (NA == 6) { RDW((cur) ^ 1, sb, ks, 3); SB(); MM1(5, xf[cur][ODD ? 1 : 0], wf[cur][3], V5); SB(); } } while (0)
  asm volatile("s_barrier" ::: "memory");
  RDX(0, lds0, 0); RDW(0, lds0, 0, 0); RDW(0, lds0, 0, 1); RDW(0, lds0, 0, 2); if (NA == 6) RDW(0, lds0, 0, 3);
  unsigned sb = lds0; int slot = 0;
#pragma unroll 1
  for (int c = 0; c < NKC; ++c) {
#pragma unroll
    for (int k = 0; k < 3; ++k) STEPG(k & 1, sb, k + 1);
    asm volatile("s_waitcnt lgkmcnt(0)\n\ts_barrier" ::: "memory");
    slot = slot + 1 == NST ? 0 : slot + 1; sb = lds0 + slot * STG;
    STEPG(1, sb, 0);
  }
#undef RDX
#undef RDW
#undef MM1
#undef STEPG
}
template <int NSTG, int STG, int NI, class Issue>
DI void gemm_load(const unsigned lds0, Issue&& issue) {
  issue(0, lds0); issue(1, lds0 + STG);
  asm volatile("s_waitcnt vmcnt(%0)" :: "n"(NI) : "memory");
  asm volatile("s_barrier" ::: "memory");
#pragma unroll
  for (int c = 2; c < NSTG; ++c) issue(c, lds0 + c * STG);
  unsigned sb = lds0; int slot = 0;
#pragma unroll 1
  for (int c = 0; c <= NKC - NSTG; ++c) {
    asm volatile("s_waitcnt vmcnt(%0)\n\ts_barrier" :: "n"(2 * NI) : "memory");
    if (c + NSTG < NKC) issue(c + NSTG, sb);
    slot = slot + 1 == NSTG ? 0 : slot + 1; sb = lds0 + slot * STG;
  }
  asm volatile("s_waitcnt vmcnt(%0)\n\ts_barrier" :: "n"(NI) : "memory");
  asm volatile("s_waitcnt vmcnt(0)\n\ts_barrier" ::: "memory");
  asm volatile("s_barrier" ::: "memory");
}

constexpr int PXB = 128 * 128, PSTG = PXB + WBYTES + EBYTES;
template <bool EX>
DI void proj_load(const char* __restrict__ xs, const char* __restrict__ w, const unsigned ldw2, const char* __restrict__ we, const int koff, const unsigned lds0, const int lw, const int lane) {
  const unsigned xe = (32 * lw + (lane >> 3)) * (D * 2) + (((lane & 7) ^ (lane >> 4)) << 4), xod = xe ^ 64;
  unsigned wo[5];
  { const unsigned q = (lane * 205) >> 12; unsigned c = lane - 20 * q, o = (16 * lw + q) * ldw2 + c * 16;
#pragma unroll
    for (int i = 0; i < 5; ++i) { wo[i] = o; c += 4; const bool cy = c >= 20; c = cy ? c - 20 : c; o += 3 * ldw2 + 64 + (cy ? ldw2 - 320 : 0); } }
  const int ep = lw < 2 ? lw : 2;
  const unsigned eo = ep * 1024 + (ep == 2 ? (lane & 31) : lane) * 16;
  auto issue = [&](int ci, unsigned sb) {
    int c = ci + koff; if (c >= NKC) c -= NKC;
    const char* xc = xs + c * (BK * 2); const char* wc = w + (size_t)c * BK * ldw2;
#pragma unroll
    for (int i = 0; i < 4; ++i) glds16s(xc + i * (8 * D * 2), (i & 1) ? xod : xe, sb + (4 * lw + i) * 1024);
#pragma unroll
    for (int i = 0; i < 5; ++i) glds16s(wc, wo[i], sb + PXB + (5 * lw + i) * 1024);
    if (EX) glds16s(we + c * 2560, eo, sb + PXB + WBYTES + ep * 1024);
  };
  gemm_load<NST, PSTG, EX ? 10 : 9>(lds0, issue);
}
template <int KIND, int EXTRA>
DI void proj_tile(int row0, char* qk_base  , size_t head_stride, int rec, char* e_base, const unsigned lds0, char* cbase, const int cheads) {
  constexpr int XBYTES = PXB, STG = PSTG;
  const int tid = threadIdx.x, lane = tid & 63, r = lane & 31, h = lane >> 5;
  const int wid = __builtin_amdgcn_readfirstlane(tid >> 6);
  const int tp = wid >> 1, odd = wid & 1;
  const unsigned xa = tp * 8192 + r * 128 + ((h ^ ((r >> 1) & 7)) << 4);
  const int g = 4 * ((lane >> 4) & 1) + (lane & 3), q = (lane & 15) >> 2;
  unsigned wa[4];
#pragma unroll
  for (int i = 0; i < 3; ++i) { const int ct = i < 2 ? 2 * odd + i : 4; wa[i] = XBYTES + (8 * h + q) * WROW + (KIND == 2 ? colmap_id(ct, g) : colmap_head(ct, g)) * 2; }
  { int col;
    if (EXTRA == 1) { const int w8 = 2 * (g >> 2) + (g & 1), u = (g >> 1) & 1; col = w8 < 2 ? 8 * w8 + 4 * u : (w8 == 2 && u == 0 ? 16 : 0); }
    else col = g < 5 ? 4 * g : 0;
    wa[3] = XBYTES + WBYTES + (8 * h + q) * 40 + col * 2; }
  constexpr int NA = EXTRA != 0 ? 6 : 5;
  f32x16 acc[6];
#pragma unroll
  for (int i = 0; i < 6; ++i) acc[i] = f32x16{};
  if (odd) gemm_consume<NA, STG, KIND == 2, EXTRA == 2, true>(acc, lds0, xa, wa);
  else gemm_consume<NA, STG, KIND == 2, EXTRA == 2, false>(acc, lds0, xa, wa);
#pragma unroll
  for (int i = 0; i < NA; ++i) {
    const bool ex = i == 5;
    const int ct = i < 4 ? 2 * odd + (i >> 1) : 4;
    const int tile = (row0 >> 5) + (i < 4 ? 2 * tp + (i & 1) : wid);
    if (ex ? (EXTRA == 2) : (KIND == 2)) {
      const int c = ex ? r : 32 * ct + r;
      const int hl = ex ? 0 : c / 20, d = ex ? r : c - 20 * hl;
      if (!ex || r < 20) {
        char* rb = (ex ? e_base : qk_base + hl * head_stride) + (size_t)tile * REC + VOFF + (h * RB) * 16;
#pragma unroll
        for (int s = 0; s < 2; ++s) {
          u32x4 v = {cvtpk(acc[i][8 * s], acc[i][8 * s + 1]), cvtpk(acc[i][8 * s + 2], acc[i][8 * s + 3]), cvtpk(acc[i][8 * s + 4], acc[i][8 * s + 5]), cvtpk(acc[i][8 * s + 6], acc[i][8 * s + 7])};
          st16<WT1>(rb + s * VS + d * 16, v);
        }
      }
    } else {
      const bool isq = !ex && KIND == 0;
      const float sc = isq ? QSCALE : 1.0f;
#pragma unroll
      for (int o = 0; o < 2; ++o) {
        u32x4 v = {cvtpk(acc[i][8 * o] * sc, acc[i][8 * o + 1] * sc), cvtpk(acc[i][8 * o + 2] * sc, acc[i][8 * o + 3] * sc),
                   cvtpk(acc[i][8 * o + 4] * sc, acc[i][8 * o + 5] * sc), cvtpk(acc[i][8 * o + 6] * sc, acc[i][8 * o + 7] * sc)};
        if (ex) {
          char* rb = e_base + (size_t)tile * REC;
          if (o == 0) st16<WT1>(rb + h * 512 + r * 16, v);
          else if (h == 0) st8<WT1>(rb + 1024 + r * 8, u32x2{v[0], v[1]});
        } else if (i < 4) {
          st16<WT1>(qk_base + (size_t)(ct + 4 * o) * head_stride + (size_t)tile * rec + h * 512 + r * 16, v);
        } else {
          char* rb = qk_base + (size_t)(4 * o + 2 * h) * head_stride + (size_t)tile * rec;
          st8<WT1>(rb + 1024 + r * 8, u32x2{v[0], v[1]});
          st8<WT1>(rb + head_stride + 1024 + r * 8, u32x2{v[2], v[3]});
        }
      }
    }
  }
  if (KIND == 0 && cbase != nullptr) {
    char* cb = cbase + (size_t)((row0 >> 5) + wid) * REC;
    if ((lane >> 3) < cheads) { const unsigned c1 = (lane & 1) ? 0u : 0x3F803F80u;
      st16<WT1>(cb + (size_t)(lane >> 3) * NT * REC + VOFF + (((lane >> 1) & 3) * RB + 20 + (lane & 1)) * 16, u32x4{c1, c1, c1, c1}); }
    if (lane < cheads) st8<WT1>(cb + (size_t)lane * NT * REC + 1280, u32x2{0x3F803F80u, 0x00003F80u});
  }
}
constexpr int PROJ_LDS = NST * PSTG + 256;

__global__ __launch_bounds__(512) void proj_kernel(const u16* __restrict__ xa, const u16* __restrict__ wqkv, const u16* __restrict__ xb, const u16* __restrict__ wq,
                                                   const u16* __restrict__ wk, const u16* __restrict__ wv, char* __restrict__ ws) {
  extern __shared__ __attribute__((aligned(16))) char lds[];
  const int xg = blockIdx.x & 7, j = blockIdx.x >> 3;
  const int rblk = (xg & 1) * 8 + (j & 7), cq = j >> 3;
  const int row0 = rblk * 128, kind = xg >> 1;
  const unsigned lds0 = (unsigned)(uintptr_t)lds;
  const int wid = __builtin_amdgcn_readfirstlane(threadIdx.x >> 6);
  if (__builtin_expect(wid >= 4, 1)) {
    const bool a = xg < 6;
    const char* xs = (const char*)(a ? xa : xb) + (size_t)row0 * D * 2;
    const char* w = (const char*)(a ? wqkv + kind * 640 : wq) + cq * 320;
    const int koff = ((j & 7) + 3 * cq) % NKC;
    if (!a && cq < 2) proj_load<true>(xs, w, 1280, (const char*)(cq ? wv : wk), koff, lds0, wid - 4, threadIdx.x & 63);
    else proj_load<false>(xs, w, a ? 3840 : 1280, nullptr, koff, lds0, wid - 4, threadIdx.x & 63);
    return;
  }
  if (wid == 0) {
    const char *pa, *pb, *pc, *pd;
    SYM_ADDR("_Z11attn_kernelPKcPcS0_S0_", pa); SYM_ADDR("_Z12oproj_kernelPKtS0_S0_Pt", pb); SYM_ADDR("_Z11proj_kernelPKtS0_S0_S0_S0_S0_Pc", pc); SYM_ADDR("_Z11attn_kernelPKcPcS0_S0_.kd", pd);
    const int i = j * 12 + (threadIdx.x & 63);
    constexpr int NA_ = 75, NB_ = 56, NC_ = 184;
    if ((threadIdx.x & 63) < 12 && i < NA_ + NB_ + NC_ + 2)
      l2touch(i < NA_ ? pa + i * 128 : i < NA_ + NB_ ? pb + (i - NA_) * 128 : i < NA_ + NB_ + NC_ ? pc + (i - NA_ - NB_) * 128 : pd - 64 + (i - NA_ - NB_ - NC_) * 64, rfl(lds0 + NST * PSTG));
  }
  char* kva = ws + WS_KVA + (size_t)(cq * 8) * NT * REC;
  if (kind == 1) proj_tile<1, 0>(row0, kva, (size_t)NT * REC, REC, nullptr, lds0, nullptr, 0);
  else if (kind == 2) proj_tile<2, 0>(row0, kva, (size_t)NT * REC, REC, nullptr, lds0, nullptr, 0);
  else {
    char* qb = ws + WS_Q + (size_t)((kind ? H : 0) + cq * 8) * NT * QREC;
    if (kind == 3 && cq == 0) proj_tile<0, 1>(row0, qb, (size_t)NT * QREC, QREC, ws + WS_KVB, lds0, nullptr, 0);
    else if (kind == 3 && cq == 1) proj_tile<0, 2>(row0, qb, (size_t)NT * QREC, QREC, ws + WS_KVB, lds0, nullptr, 0);
    else proj_tile<0, 0>(row0, qb, (size_t)NT * QREC, QREC, nullptr, lds0, kind == 0 ? kva : (cq == 2 ? ws + WS_KVB : nullptr), kind == 0 ? 8 : 1);
  }
}

constexpr int ONST = 4, OBK = 80, ONKC = D / OBK;
constexpr int OXB = 10 * 1024;
constexpr int OSTG = OXB + OBK * WROW;
constexpr int OPROJ_LDS = ONST * OSTG;
template <int NTL>
DI void oproj_consume(f32x16 (&acc)[3], const unsigned lds0, const unsigned xa, const unsigned (&wa)[3]) {
  bf16x8 xf[2], wf[2][3];
#define RDX(set, sb, ks) xf[set] = *(const LDSP bf16x8*)((sb) + xa + (ks) * 2048)
#define RDW(set, sb, ks, i) do { const unsigned a_ = (sb) + wa[i] + (16 * (ks)) * WROW; \
      const bf16x4 lo_ = trread(a_), hi_ = trread(a_ + 4 * WROW); wf[set][i] = __builtin_shufflevector(lo_, hi_, 0, 1, 2, 3, 4, 5, 6, 7); } while (0)
#define STEPG(cur, sb, ks) do { \
    acc[0] = MFMA16(wf[cur][0], xf[cur], acc[0]); SB(); RDX((cur) ^ 1, sb, ks); RDW((cur) ^ 1, sb, ks, 0); SB(); \
    acc[1] = MFMA16(wf[cur][1], xf[cur], acc[1]); SB(); RDW((cur) ^ 1, sb, ks, 1); SB(); \
    if (NTL == 3) { acc[2] = MFMA16(wf[cur][2], xf[cur], acc[2]); SB(); RDW((cur) ^ 1, sb, ks, 2); SB(); } } while (0)
  asm volatile("s_barrier" ::: "memory");
  RDX(0, lds0, 0); RDW(0, lds0, 0, 0); RDW(0, lds0, 0, 1); if (NTL == 3) RDW(0, lds0, 0, 2);
  unsigned sb = lds0; int slot = 0;
#define CHUNK(p) do { \
    STEPG(p, sb, 1); STEPG((p) ^ 1, sb, 2); STEPG(p, sb, 3); STEPG((p) ^ 1, sb, 4); \
    asm volatile("s_waitcnt lgkmcnt(0)\n\ts_barrier" ::: "memory");        \
    slot = slot + 1 == ONST ? 0 : slot + 1; sb = lds0 + slot * OSTG; \
    STEPG(p, sb, 0); } while (0)
#pragma unroll 1
  for (int c = 0; c < ONKC; c += 2) { CHUNK(0); CHUNK(1); }
#undef CHUNK
#undef RDX
#undef RDW
#undef STEPG
}
__global__ __launch_bounds__(512) void oproj_kernel(const u16* __restrict__ o, const u16* __restrict__ woa, const u16* __restrict__ wob, u16* __restrict__ out) {
  extern __shared__ __attribute__((aligned(16))) char lds[];
  const int tid = threadIdx.x, lane = tid & 63, r = lane & 31, h = lane >> 5;
  const int wid = __builtin_amdgcn_readfirstlane(tid >> 6);
  const int xg = blockIdx.x & 7, j = blockIdx.x >> 3;
  const int br = xg >> 2, rblk = (xg & 3) * 8 + (j & 7), cq = j >> 3;
  const int row0 = rblk * 64;
  const unsigned lds0 = (unsigned)(uintptr_t)lds;
  if (__builtin_expect(wid >= 4, 1)) {
    const int lw = wid - 4;
    const int x0 = lw < 2 ? 3 * lw : 2 * lw + 2, w0 = lw < 2 ? 6 * lw : 7 * lw - 2;
    const unsigned xl = (lane >> 5) * OREC + (lane & 31) * 16;
    unsigned wv[7];
    { const unsigned lq = (lane * 205) >> 12;
      const int u0 = 64 * w0, r0 = u0 / 20, m0 = u0 - 20 * r0;
      unsigned c16 = m0 + lane - 20 * lq, kk = r0 + lq; { const unsigned cy = c16 >= 20; c16 -= 20 * cy; kk += cy; }
#pragma unroll
      for (int i = 0; i < 7; ++i) {
        const unsigned sl = kk >> 3, ii = kk & 7, up = sl >= 5, e = sl - 5 * up;
        const unsigned ka = 20 * (e >> 1) + 8 * (e & 1) + ii, kb = 16 + ii + ((ii & 4) << 2);
        wv[i] = (40 * up + (e == 4 ? kb : ka)) * (D * 2) + c16 * 16;
        const unsigned adv = (w0 + i < 24);
        c16 += 4 * adv; const unsigned cy = c16 >= 20; c16 -= 20 * cy; kk += 3 * adv + cy;
      } }
    const char* xs = (const char*)o + ((size_t)br * 16 * NT + (row0 >> 5)) * OREC;
    const char* w = (const char*)((br ? wob : woa) + cq * 160);
    auto issue = [&](int c, unsigned sb) {
      const char* xc = xs + (size_t)c * (2 * NT * OREC); const char* wc = w + (size_t)c * OBK * D * 2;
#pragma unroll
      for (int i = 0; i < 2; ++i) { const int q = x0 + i; glds16s(xc + (q >= 5 ? NT * OREC - 5 * 512 : 0) + q * 512, xl, sb + q * 1024); }
      if (lw < 2) { const int q = x0 + 2; glds16s(xc + (q >= 5 ? NT * OREC - 5 * 512 : 0) + q * 512, xl, sb + q * 1024); }
      else glds16s(wc, wv[0], sb + OXB + w0 * 1024);
#pragma unroll
      for (int i = 0; i < 6; ++i) { const int iw = i + (lw < 2 ? 0 : 1); glds16s(wc, lw < 2 ? wv[i] : wv[i + 1], sb + OXB + min(w0 + iw, 24) * 1024); }
    };
    issue(0, lds0); issue(1, lds0 + OSTG);
    asm volatile("s_waitcnt vmcnt(9)\n\ts_barrier" ::: "memory");
    issue(2, lds0 + 2 * OSTG); issue(3, lds0 + 3 * OSTG);
    unsigned sb = lds0; int slot = 0;
#pragma unroll 1
    for (int c = 0; c <= ONKC - ONST; ++c) {
      asm volatile("s_waitcnt vmcnt(18)\n\ts_barrier" ::: "memory");
      if (c + ONST < ONKC) issue(c + ONST, sb);
      slot = slot + 1 == ONST ? 0 : slot + 1; sb = lds0 + slot * OSTG;
    }
    asm volatile("s_waitcnt vmcnt(9)\n\ts_barrier" ::: "memory");
    asm volatile("s_waitcnt vmcnt(0)\n\ts_barrier" ::: "memory");
    asm volatile("s_barrier" ::: "memory");
  } else {
    const int tt = wid & 1, cg = wid >> 1;
    const unsigned xa = h * 1024 + (32 * tt + r) * 16;
    const int g = 4 * ((lane >> 4) & 1) + (lane & 3), q4 = (lane & 15) >> 2;
    unsigned wa[3];
#pragma unroll
    for (int i = 0; i < 3; ++i) wa[i] = OXB + (8 * h + q4) * WROW + colmap_oct(cg * 3 + (i < 2 || cg == 0 ? i : 0), g) * 2;
    f32x16 acc[3];
#pragma unroll
    for (int i = 0; i < 3; ++i) acc[i] = f32x16{};
    if (cg == 0) oproj_consume<3>(acc, lds0, xa, wa); else oproj_consume<2>(acc, lds0, xa, wa);
    const unsigned sa = lds0 + (32 * tt + r) * 336 + (96 * cg + 8 * h) * 2;
#pragma unroll
    for (int i = 0; i < 3; ++i) {
      if (i == 2 && cg == 1) continue;
#pragma unroll
      for (int o8 = 0; o8 < 2; ++o8)
        *(LDSP u32x4*)(sa + (32 * i + 16 * o8) * 2) = u32x4{cvtpk(acc[i][8 * o8], acc[i][8 * o8 + 1]), cvtpk(acc[i][8 * o8 + 2], acc[i][8 * o8 + 3]), cvtpk(acc[i][8 * o8 + 4], acc[i][8 * o8 + 5]), cvtpk(acc[i][8 * o8 + 6], acc[i][8 * o8 + 7])};
    }
  }
  asm volatile("s_waitcnt lgkmcnt(0)\n\ts_barrier" ::: "memory");
  char* ob = (char*)(out + (size_t)row0 * 1280 + br * 640 + cq * 160);
#pragma unroll
  for (int k = 0; k < 3; ++k) {
    const int p = tid + 512 * k;
    if (p < 1280) { const int row = (p * 3277) >> 16, c = p - 20 * row;
      st16<WT3>(ob + (size_t)row * 2560 + c * 16, *(const LDSP u32x4*)(lds0 + row * 336 + c * 16)); }
  }
}

#define MX3(a, b, c) __builtin_fmaxf(__builtin_fmaxf((a), (b)), (c))
DI float rowmax16(const f32x16& p) {
  float a = MX3(p[0], p[1], p[2]), b = MX3(p[3], p[4], p[5]);
  a = MX3(a, p[6], p[7]); b = MX3(b, p[8], p[9]); a = MX3(a, p[10], p[11]); b = MX3(b, p[12], p[13]); a = MX3(a, p[14], p[15]);
  return __builtin_fmaxf(a, b);
}
DI float xhalf_max(float m) {
  auto rr = __builtin_amdgcn_permlane32_swap(__float_as_uint(m), __float_as_uint(m), false, false);
  return __builtin_fmaxf(__uint_as_float(rr[0]), __uint_as_float(rr[1]));
}
DI bf16x4 negm_frag(float m) {
  const float n = -m;
  const unsigned h1 = cvtpk(n, n) & 0xffffu; const float r1 = n - __uint_as_float(h1 << 16);
  const unsigned h2 = cvtpk(r1, r1) & 0xffffu; const float r2 = r1 - __uint_as_float(h2 << 16);
  const unsigned h3 = cvtpk(r2, r2) & 0xffffu;
  u32x2 p = {h1 | (h2 << 16), h3};
  return __builtin_bit_cast(bf16x4, p);
}

constexpr int AW = 8;
constexpr int TPC = 16;
constexpr int CH = TPC * REC;
constexpr int NCH = NT / TPC;
constexpr int NBUF = 3;
constexpr int QL_OFF = NBUF * CH + 64;
constexpr int ATT_LDS = QL_OFF + 16 * QREC + 256;

__global__ __launch_bounds__(64 * (AW + 1)) void attn_kernel(const char* __restrict__ ws_ro, char* __restrict__ ws, const char* __restrict__ woa, const char* __restrict__ wob) {
  extern __shared__ __attribute__((aligned(16))) char lds[];
  const int tid = threadIdx.x, lane = tid & 63, r = lane & 31, h = lane >> 5;
  const int wid = __builtin_amdgcn_readfirstlane(tid >> 6);
  const int xg = blockIdx.x & 7, j = blockIdx.x >> 3;
  const int br = j >> 4, head = 4 * xg + ((j >> 2) & 3), rb = j & 3;
  const char* kv = ws_ro + (br ? WS_KVB : WS_KVA + (size_t)head * NT * REC);
  const char* qp = ws_ro + WS_Q + ((size_t)(br * H + head) * NT + rb * 16) * QREC;
  const unsigned lds0 = (unsigned)(uintptr_t)lds;
  volatile LDSP int* flag = (volatile LDSP int*)(lds0 + NBUF * CH);
#define DMA_REC(s_, d_) do { glds16s(s_, lane * 16, d_); glds16s((s_) + 1024, lane * 16, (d_) + 1024); if (lane < 41) glds16s((s_) + 2048, lane * 16, (d_) + 2048); } while (0)
  if (wid == AW) {
    for (int attempt = 0; attempt < 2; ++attempt) {
#pragma unroll 1
      for (int t = 8; t < TPC; ++t) DMA_REC(kv + t * REC, lds0 + t * REC);
      asm volatile("s_barrier" ::: "memory");
#pragma unroll 1
      for (int t = 0; t < 13; ++t) DMA_REC(kv + CH + t * REC, lds0 + CH + t * REC);
      asm volatile("s_waitcnt vmcnt(39)\n\ts_barrier" ::: "memory");
#pragma unroll 1
      for (int t = 13; t < TPC; ++t) DMA_REC(kv + CH + t * REC, lds0 + CH + t * REC);
#pragma unroll 1
      for (int c = 0; c < NCH; ++c) {
        asm volatile("s_waitcnt vmcnt(0)\n\ts_barrier" ::: "memory");
        if (c + 2 < NCH) {
          const char* src = kv + (size_t)(c + 2) * CH; const unsigned dst = lds0 + ((c + 2) % NBUF) * CH;
#pragma unroll 1
          for (int t = 0; t < TPC; ++t) DMA_REC(src + t * REC, dst + t * REC);
        }
        if (lane < 50) l2touch((xg >> 2 ? wob : woa) + (size_t)(j * 200 + c * 50 + lane) * 128, rfl(lds0 + QL_OFF + 16 * QREC));
      }
      {
        const char* pb; SYM_ADDR("_Z12oproj_kernelPKtS0_S0_Pt", pb);
        if (lane < 56) l2touch(pb + lane * 128, rfl(lds0 + QL_OFF + 16 * QREC)); }
      if (attempt == 1) break;
      asm volatile("s_barrier" ::: "memory");
      if (*flag == 0) break;
      asm volatile("s_waitcnt vmcnt(0) lgkmcnt(0)\n\ts_barrier" ::: "memory");
    }
    return;
  }
  const char* dsrc = kv + wid * REC;
  const unsigned ddst = rfl(lds0 + wid * REC);

  const int rm = r <= 21 ? r : 21;
  const unsigned ak0 = h * 512 + r * 16, ak1 = h ? 1280 : 1024 + r * 8, av = VOFF + (h * RB + rm) * 16;

  glds16s(qp + wid * 2560, lane * 16, rfl(lds0 + QL_OFF + wid * 2560)); glds16s(qp + wid * 2560 + 1024, lane * 16, rfl(lds0 + QL_OFF + wid * 2560 + 1024));
  if (lane < 32) glds16s(qp + wid * 2560 + 2048, lane * 16, rfl(lds0 + QL_OFF + wid * 2560 + 2048));
  if (tid == 0) *flag = 0;
  DMA_REC(dsrc, ddst);
  bf16x8 qa0, qb0; bf16x4 qa1, qb1;
  f32x16 oa, ob;
  for (int attempt = 0; attempt < 2; ++attempt) {
    asm volatile("s_waitcnt vmcnt(0) lgkmcnt(0)\n\ts_barrier" ::: "memory");
    f32x16 sa, sb; bf16x8 k0, v0, v1; bf16x4 k1; u32x4 pa0, pa1, pb0, pb1;
    {
      const unsigned ql = lds0 + QL_OFF + wid * 2560;
      qa0 = *(const LDSP bf16x8*)(ql + h * 512 + r * 16); const bf16x4 qa1d = *(const LDSP bf16x4*)(ql + 1024 + r * 8);
      qb0 = *(const LDSP bf16x8*)(ql + 1280 + h * 512 + r * 16); const bf16x4 qb1d = *(const LDSP bf16x4*)(ql + 1280 + 1024 + r * 8);
      const bf16x4 z4 = {0, 0, 0, 0};
      const bf16x4 qaz = h ? z4 : qa1d, qbz = h ? z4 : qb1d;
      k0 = *(const LDSP bf16x8*)(lds0 + ak0); k1 = *(const LDSP bf16x4*)(lds0 + ak1);
      v0 = *(const LDSP bf16x8*)(lds0 + av);
      sa = MFMA16(k0, qa0, f32x16{}); sb = MFMA16(k0, qb0, f32x16{});
      sa = MFMA8(k1, qaz, sa); sb = MFMA8(k1, qbz, sb);
      float ma, mb;
      if (attempt == 0) {
        ma = rowmax16(sa); mb = rowmax16(sb);
      } else {
        ma = -INFINITY; mb = -INFINITY;
        for (int t = 0; t < NT; ++t) {
          const char* rec = kv + (size_t)t * REC;
          const bf16x8 kf0 = *(const bf16x8*)(rec + ak0); const bf16x4 kf1 = *(const bf16x4*)(rec + ak1);
          f32x16 s = MFMA16(kf0, qa0, f32x16{}); s = MFMA8(kf1, qaz, s); ma = __builtin_fmaxf(ma, rowmax16(s));
          s = MFMA16(kf0, qb0, f32x16{}); s = MFMA8(kf1, qbz, s); mb = __builtin_fmaxf(mb, rowmax16(s));
        }
      }
      ma = xhalf_max(ma); mb = xhalf_max(mb);
#pragma unroll
      for (int i = 0; i < 16; ++i) sa[i] -= ma;
      qa1 = h ? negm_frag(ma) : qa1d; qb1 = h ? negm_frag(mb) : qb1d;
    }
    oa = f32x16{}; ob = f32x16{};
    v1 = v0; pb0 = u32x4{0, 0, 0, 0}; pb1 = pb0;
#define EX(x) __builtin_amdgcn_exp2f(x)
#define BF(x) __builtin_bit_cast(bf16x8, x)
#define GAP(SC, i, P, k, M) do { float f0 = EX(SC[i]), f1 = EX(SC[i + 1]); PIN(f0); PIN(f1); SB(); M; SB(); \
      const float f2 = EX(SC[i + 2]), f3 = EX(SC[i + 3]); P[k] = cvtpk(f0, f1); P[k + 1] = cvtpk(f2, f3); PIN(P); SB(); } while (0)
#define STEP(KN0, KN1, VC) do { \
      GAP(sa, 0, pa0, 0, { sb = MFMA16(k0, qb0, f32x16{}); SB(); k0 = *(const LDSP bf16x8*)(KN0); }); \
      GAP(sa, 4, pa0, 2, { sb = MFMA8(k1, qb1, sb); SB(); k1 = *(const LDSP bf16x4*)(KN1); }); \
      GAP(sa, 8, pa1, 0, { ob = MFMA16(v0, BF(pb0), ob); SB(); v0 = *(const LDSP bf16x8*)(VC); }); \
      GAP(sa, 12, pa1, 2, { ob = MFMA16(v1, BF(pb1), ob); SB(); v1 = *(const LDSP bf16x8*)((VC) + VS); }); \
      GAP(sb, 0, pb0, 0, { sa = MFMA16(k0, qa0, f32x16{}); }); \
      GAP(sb, 4, pb0, 2, { sa = MFMA8(k1, qa1, sa); }); \
      GAP(sb, 8, pb1, 0, { oa = MFMA16(v0, BF(pa0), oa); }); \
      GAP(sb, 12, pb1, 2, { oa = MFMA16(v1, BF(pa1), oa); }); \
    } while (0)
    for (int c = 0; c < NCH; ++c) {
      const unsigned base = lds0 + (c % NBUF) * CH;
      const unsigned basen = lds0 + ((c + 1) % NBUF) * CH;
      const LDSP char* pk0 = (const LDSP char*)(base + ak0);
      const LDSP char* pk1 = (const LDSP char*)(base + ak1);
      const LDSP char* pv = (const LDSP char*)(base + av);
      const LDSP char* nk0 = (const LDSP char*)(basen + ak0);
      const LDSP char* nk1 = (const LDSP char*)(basen + ak1);
#pragma unroll
      for (int t = 0; t < TPC; ++t) {
        if (t == 4 && c == 0) asm volatile("s_barrier" ::: "memory");
        if (t == TPC / 2) asm volatile("s_barrier" ::: "memory");
        STEP(t + 1 < TPC ? pk0 + (t + 1) * REC : nk0, t + 1 < TPC ? pk1 + (t + 1) * REC : nk1, pv + t * REC);
      }
    }
    ob = MFMA16(v0, BF(pb0), ob); ob = MFMA16(v1, BF(pb1), ob);
#undef EX
#undef GAP
#undef BF
#undef STEP
    bool bad = false;
#pragma unroll
    for (int qb = 0; qb < 2; ++qb) {
      const f32x16& o = qb ? ob : oa;
      auto rr = __builtin_amdgcn_permlane32_swap(__float_as_uint(o[8]), __float_as_uint(o[8]), false, false);
      const float l = __uint_as_float(rr[1]);
      bad |= !(l <= 1.152921504606847e18f);
      const float inv = __builtin_amdgcn_rcpf(l);
      char* op = ws + WS_O + (((size_t)br * 16 + (head >> 1)) * NT + rb * 16 + wid * 2 + qb) * OREC + r * 16;
      const auto sx = __builtin_amdgcn_permlane32_swap(cvtpk(o[0] * inv, o[1] * inv), cvtpk(o[4] * inv, o[5] * inv), false, false);
      const auto sy = __builtin_amdgcn_permlane32_swap(cvtpk(o[2] * inv, o[3] * inv), cvtpk(o[6] * inv, o[7] * inv), false, false);
      st16<WT2>(op + ((head & 1) * 2 + h) * 512, u32x4{sx[0], sy[0], sx[1], sy[1]});
      if (h == 0) st8<WT2>(op + 2048 + (head & 1) * 8, u32x2{cvtpk(o[8] * inv, o[9] * inv), cvtpk(o[10] * inv, o[11] * inv)});
    }
    if (attempt == 1) break;
    if (__any(bad)) *flag = 1;
    asm volatile("s_waitcnt lgkmcnt(0)\n\ts_barrier" ::: "memory");
    if (*flag == 0) break;
    asm volatile("s_waitcnt vmcnt(0) lgkmcnt(0)\n\ts_barrier" ::: "memory");
    DMA_REC(dsrc, ddst);
  }
#undef DMA_REC
}

extern "C" void kernel_launch(void* const* d_in, const int* in_sizes, int n_in, void* d_out, int out_size, void* d_ws, size_t ws_size, hipStream_t stream) {
  const u16* xa = (const u16*)d_in[0]; const u16* wqkv = (const u16*)d_in[1]; const u16* woa = (const u16*)d_in[2];
  const u16* xb = (const u16*)d_in[3]; const u16* wq = (const u16*)d_in[4]; const u16* wk = (const u16*)d_in[5];
  const u16* wv = (const u16*)d_in[6]; const u16* wob = (const u16*)d_in[7];
  char* ws = (char*)d_ws;
  (void)hipFuncSetAttribute((const void*)proj_kernel, hipFuncAttributeMaxDynamicSharedMemorySize, PROJ_LDS);
  (void)hipFuncSetAttribute((const void*)attn_kernel, hipFuncAttributeMaxDynamicSharedMemorySize, ATT_LDS);
  (void)hipFuncSetAttribute((const void*)oproj_kernel, hipFuncAttributeMaxDynamicSharedMemorySize, OPROJ_LDS);
  proj_kernel<<<256, 512, PROJ_LDS, stream>>>(xa, wqkv, xb, wq, wk, wv, ws);
  attn_kernel<<<256, 64 * (AW + 1), ATT_LDS, stream>>>(ws, ws, (const char*)woa, (const char*)wob);
  oproj_kernel<<<256, 512, OPROJ_LDS, stream>>>((const u16*)(ws + WS_O), woa, wob, (u16*)d_out);
}
```
